```python
import torch

def _softmax(x, axis=-1):
    return torch.softmax(x, dim=axis)
def _gelu(x):
    return torch.nn.functional.gelu(x, approximate="tanh")
def _silu(x):
    return torch.nn.functional.silu(x)
def _sigmoid(x):
    return torch.sigmoid(x)
def _layernorm(x, axis=-1, eps=1e-5):
    m = x.mean(dim=axis, keepdim=True)
    v = x.var(dim=axis, keepdim=True, unbiased=False)
    return (x - m) / torch.sqrt(v + eps)
def _scatter_add(tgt, idx, src, axis=0):
    return tgt.clone().index_add_(axis, idx, src)
def _segment_sum(src, seg, n):
    out = torch.zeros((n,) + src.shape[1:], dtype=src.dtype)
    return out.index_add_(0, seg, src)
def _pad(x, pw):
    flat = tuple(v for pair in reversed(pw) for v in pair)
    return torch.nn.functional.pad(x, flat)

kernel_name = 'standalone__window_attn_swin__vision'

def setup_inputs():

    _w = (lambda a: (lambda t: t.to(torch.bfloat16) if t.is_floating_point() else t)(torch.as_tensor(a)))
    H = W = 384
    C = 192
    ws = 8
    nh = 4
    shift = ws // 2
    coords = torch.stack(torch.meshgrid(torch.arange(ws), torch.arange(ws), indexing='ij'), 0).reshape(2, -1)
    rel = coords[:, :, None] - coords[:, None, :]
    rel[0] += ws - 1
    rel[1] += ws - 1
    rel[0] *= 2 * ws - 1
    rp_idx = rel.sum(0).to(torch.int64)
    img_mask = torch.zeros((1, H, W, 1), dtype=float)
    cnt = 0
    for hs in (slice(0, H - ws), slice(H - ws, H - shift), slice(H - shift, H)):
        for vs in (slice(0, W - ws), slice(W - ws, W - shift), slice(W - shift, W)):
            img_mask[:, hs, vs, :] = cnt
            cnt += 1
    mw = img_mask.reshape(1, H // ws, ws, W // ws, ws, 1).permute(0, 1, 3, 2, 4, 5).reshape(-1, ws * ws)
    am = mw[:, :, None] - mw[:, None, :]
    attn_mask = torch.where(am != 0, -100.0, 0.0).to(float)
    return {
        "x": _w((lambda *s: torch.randn(*s, dtype=torch.bfloat16) * 1.6)(1, H, W, C)),
        "wqkv": _w((lambda *s: torch.randn(*s, dtype=torch.bfloat16) * 1.6)(C, 3 * C) * 0.02),
        "wo": _w((lambda *s: torch.randn(*s, dtype=torch.bfloat16) * 0.05)(C, C) * 0.02),
        "rpb": _w((lambda *s: torch.randn(*s, dtype=torch.bfloat16) * 0.2)((2 * ws - 1) * (2 * ws - 1), nh)),
        "rp_idx": _w(rp_idx),
        "attn_mask": _w(attn_mask),
    }

def reference(x, wqkv, wo, rpb, rp_idx, attn_mask):
    B, H, W, C = x.shape
    N = rp_idx.shape[0]
    ws = int(round(N ** 0.5))
    nh = rpb.shape[-1]
    hd = C // nh
    shift = ws // 2
    xs = torch.roll(x, (-shift, -shift), (1, 2))
    nWh = H // ws
    nWw = W // ws
    xw = (lambda x, *d: x.permute(*d))(xs.reshape(B, nWh, ws, nWw, ws, C), 0, 1, 3, 2, 4, 5).reshape(B * nWh * nWw, N, C)
    qkv = (lambda x, *d: x.permute(*d))(torch.einsum('wnc,cd->wnd', xw, wqkv).reshape(-1, N, 3, nh, hd), 2, 0, 3, 1, 4)
    q, k, v = qkv[0], qkv[1], qkv[2]
    attn = torch.einsum('whid,whjd->whij', q, k) * (hd ** -0.5)
    bias = (lambda x, *d: x.permute(*d))(rpb[rp_idx.reshape(-1)].reshape(N, N, nh), 2, 0, 1)
    attn = attn + bias[None, :, :, :]
    attn = (attn.reshape(B, nWh * nWw, nh, N, N) + attn_mask[None, :, None, :, :]).reshape(-1, nh, N, N)
    attn = _softmax(attn, axis=-1)
    out = (lambda x, *d: x.permute(*d))(torch.einsum('whij,whjd->whid', attn, v), 0, 2, 1, 3).reshape(-1, N, C)
    out = torch.einsum('wnc,cd->wnd', out, wo)
    out = (lambda x, *d: x.permute(*d))(out.reshape(B, nWh, nWw, ws, ws, C), 0, 1, 3, 2, 4, 5).reshape(B, H, W, C)
    return torch.roll(out, (shift, shift), (1, 2))
```

```cpp
#include <hip/hip_runtime.h>
#include <hip/hip_bf16.h>
typedef unsigned short u16;
__device__ __forceinline__ float bf2f(u16 v){ return __uint_as_float(((unsigned)v)<<16); }
__device__ __forceinline__ u16 f2bf(float f){ __hip_bfloat16 b = __float2bfloat16(f); return *(u16*)&b; }
__device__ __forceinline__ float rbf(float f){ return bf2f(f2bf(f)); }

__global__ __launch_bounds__(256) void simple_kernel(const u16* __restrict__ x, const u16* __restrict__ wqkv, const u16* __restrict__ wo,
    const u16* __restrict__ rpb, const int* __restrict__ rpi, const u16* __restrict__ mask, u16* __restrict__ out) {
  __shared__ float xs[64*193];
  __shared__ float qkv[64*145];
  __shared__ float os[64*193];
  __shared__ float ps[64*65];
  const int w = blockIdx.x, wy = w/48, wx = w%48, tid = threadIdx.x;
  for (int e = tid; e < 64*192; e += 256) { int t = e/192, c = e%192; int y = (8*wy+4+(t>>3))%384, xx = (8*wx+4+(t&7))%384; xs[t*193+c] = bf2f(x[(y*384+xx)*192+c]); }
  __syncthreads();
  for (int h = 0; h < 4; ++h) {
    for (int e = tid; e < 64*144; e += 256) { int t = e/144, j = e%144; int col = (j/48)*192 + h*48 + (j%48); float a = 0.f;
      for (int c = 0; c < 192; ++c) a += xs[t*193+c]*bf2f(wqkv[c*576+col]);
      qkv[t*145+j] = rbf(a); }
    __syncthreads();
    for (int e = tid; e < 64*64; e += 256) { int i = e>>6, j = e&63; float a = 0.f;
      for (int d = 0; d < 48; ++d) a += qkv[i*145+d]*qkv[j*145+48+d];
      ps[i*65+j] = a*0.14433756729740643f + bf2f(rpb[rpi[e]*4+h]) + bf2f(mask[w*4096+e]); }
    __syncthreads();
    if (tid < 64) { float m = -INFINITY; for (int j = 0; j < 64; ++j) m = fmaxf(m, ps[tid*65+j]); float s = 0.f;
      for (int j = 0; j < 64; ++j) { float p = __expf(ps[tid*65+j]-m); ps[tid*65+j] = p; s += p; }
      float r = 1.f/s; for (int j = 0; j < 64; ++j) ps[tid*65+j] = rbf(ps[tid*65+j]*r); }
    __syncthreads();
    for (int e = tid; e < 64*48; e += 256) { int i = e/48, d = e%48; float a = 0.f;
      for (int j = 0; j < 64; ++j) a += ps[i*65+j]*qkv[j*145+96+d];
      os[i*193+h*48+d] = rbf(a); }
    __syncthreads();
  }
  for (int e = tid; e < 64*192; e += 256) { int t = e/192, c = e%192; float a = 0.f;
    for (int d = 0; d < 192; ++d) a += os[t*193+d]*bf2f(wo[d*192+c]);
    int y = (8*wy+4+(t>>3))%384, xx = (8*wx+4+(t&7))%384; out[(y*384+xx)*192+c] = f2bf(a); }
}
extern "C" void kernel_launch(void* const* d_in, const int* in_sizes, int n_in, void* d_out, int out_size, void* d_ws, size_t ws_size, hipStream_t stream) {
  simple_kernel<<<2304,256,0,stream>>>((const u16*)d_in[0],(const u16*)d_in[1],(const u16*)d_in[2],(const u16*)d_in[3],(const int*)d_in[4],(const u16*)d_in[5],(u16*)d_out);
}
```

```cpp
#include <hip/hip_runtime.h>
#include <hip/hip_bf16.h>
typedef unsigned short u16;
#define STAMP 0

__device__ __forceinline__ float bf2f(u16 v) { return __uint_as_float(((unsigned)v) << 16); }
__device__ __forceinline__ u16 f2bf(float f) { __hip_bfloat16 b = __float2bfloat16(f); return *(u16*)&b; }
__device__ __forceinline__ float rbf(float f) { return bf2f(f2bf(f)); }

__device__ __forceinline__ void window_true_max(float* lds, const int tid, int wy, int wx, const u16* __restrict__ x, const u16* __restrict__ wqkv,
    const u16* __restrict__ wo, const u16* __restrict__ rpb, const int* __restrict__ rpi, const u16* __restrict__ mask, u16* __restrict__ out) {
  float* xs = lds; float* qkv = xs + 64 * 193; float* os = qkv + 64 * 145; float* ps = os + 64 * 193;
  const int w = wy * 48 + wx;
  __syncthreads();
  for (int e = tid; e < 64 * 192; e += 256) { int t = e / 192, c = e % 192; int y = (8 * wy + 4 + (t >> 3)) % 384, xx = (8 * wx + 4 + (t & 7)) % 384; xs[t * 193 + c] = bf2f(x[(y * 384 + xx) * 192 + c]); }
  __syncthreads();
  for (int h = 0; h < 4; ++h) {
    for (int e = tid; e < 64 * 144; e += 256) { int t = e / 144, j = e % 144; int col = (j / 48) * 192 + h * 48 + (j % 48); float a = 0.f;
      for (int c = 0; c < 192; ++c) a += xs[t * 193 + c] * bf2f(wqkv[c * 576 + col]);
      qkv[t * 145 + j] = rbf(a); }
    __syncthreads();
    for (int e = tid; e < 64 * 64; e += 256) { int i = e >> 6, j = e & 63; float a = 0.f;
      for (int d = 0; d < 48; ++d) a += qkv[i * 145 + d] * qkv[j * 145 + 48 + d];
      ps[i * 65 + j] = a * 0.14433756729740643f + bf2f(rpb[rpi[e] * 4 + h]) + bf2f(mask[w * 4096 + e]); }
    __syncthreads();
    if (tid < 64) { float m = -INFINITY; for (int j = 0; j < 64; ++j) m = fmaxf(m, ps[tid * 65 + j]); float s = 0.f;
      for (int j = 0; j < 64; ++j) { float p = __expf(ps[tid * 65 + j] - m); ps[tid * 65 + j] = p; s += p; }
      float r = 1.f / s; for (int j = 0; j < 64; ++j) ps[tid * 65 + j] = rbf(ps[tid * 65 + j] * r); }
    __syncthreads();
    for (int e = tid; e < 64 * 48; e += 256) { int i = e / 48, d = e % 48; float a = 0.f;
      for (int j = 0; j < 64; ++j) a += ps[i * 65 + j] * qkv[j * 145 + 96 + d];
      os[i * 193 + h * 48 + d] = rbf(a); }
    __syncthreads();
  }
  for (int e = tid; e < 64 * 192; e += 256) { int t = e / 192, c = e % 192; float a = 0.f;
    for (int d = 0; d < 192; ++d) a += os[t * 193 + d] * bf2f(wo[d * 192 + c]);
    int y = (8 * wy + 4 + (t >> 3)) % 384, xx = (8 * wx + 4 + (t & 7)) % 384; out[(y * 384 + xx) * 192 + c] = f2bf(a); }
}

__global__ __launch_bounds__(256, 1) void swin_window_attn_kernel(const u16* x, const u16* wqkv, const u16* wo, const u16* rpb,
    const int* rpi, const u16* mask, u16* out, unsigned* dbg) {
  __shared__ __attribute__((aligned(16))) float lds[40960];
  const unsigned wv = __builtin_amdgcn_readfirstlane(threadIdx.x >> 6);
  const unsigned blk = blockIdx.x;
  const unsigned long long karg = (unsigned long long)__builtin_amdgcn_kernarg_segment_ptr();
  unsigned bad;
  asm volatile(
    "s_load_dwordx2 s[36:37], %1, 0\n"
    "s_load_dwordx2 s[48:49], %1, 8\n"
    "s_load_dwordx2 s[70:71], %1, 16\n"
    "s_load_dwordx4 s[72:75], %1, 24\n"
    "s_load_dwordx2 s[44:45], %1, 40\n"
    "s_load_dwordx2 s[40:41], %1, 48\n"
    "s_mov_b32 s26, %2\n"
    "s_mov_b32 s97, %3\n"
    "v_mbcnt_lo_u32_b32 v232, -1, 0\n"
    "v_mbcnt_hi_u32_b32 v232, -1, v232\n"
    "v_lshlrev_b32 v250, 4, v232\n"
    "s_lshl_b32 s88, s26, 11\n"
    "s_lshl_b32 s34, s26, 12\n"
    "s_mul_i32 s87, s26, 6144\n"
    "s_mul_i32 s31, s26, 96\n"
    "s_mul_i32 s86, s26, 294912\n"
    "s_sub_u32 s85, %3, 256\n"
    "s_add_u32 s85, s85, 256\n"
    "s_mul_hi_u32 s28, s85, 91382283\n"
    "s_mul_i32 s29, s28, 47\n"
    "s_sub_u32 s29, s85, s29\n"
    "s_mul_i32 s60, s28, 1179648\n"
    "s_mul_i32 s30, s29, 3072\n"
    "s_add_u32 s60, s60, s30\n"
    "s_add_u32 s60, s60, 591360\n"
    "s_mul_i32 s61, s28, 48\n"
    "s_add_u32 s61, s61, s29\n"
    "s_lshl_b32 s61, s61, 13\n"
    "s_add_u32 s61, s61, s88\n"
    "v_lshrrev_b32 v226, 3, v232\n"
    "v_add_u32 v223, 0, v226\n"
    "v_mul_u32_u24 v223, 43, v223\n"
    "v_lshrrev_b32 v223, 7, v223\n"
    "v_lshlrev_b32 v223, 4, v223\n"
    "v_xor_b32 v251, v223, v250\n"
    "v_add_u32 v223, 8, v226\n"
    "v_mul_u32_u24 v223, 43, v223\n"
    "v_lshrrev_b32 v223, 7, v223\n"
    "v_lshlrev_b32 v223, 4, v223\n"
    "v_xor_b32 v249, v223, v250\n"
    "v_add_u32 v223, 16, v226\n"
    "v_mul_u32_u24 v223, 43, v223\n"
    "v_lshrrev_b32 v223, 7, v223\n"
    "v_lshlrev_b32 v223, 4, v223\n"
    "v_xor_b32 v247, v223, v250\n"
    "v_add_u32 v223, 0, v232\n"
    "v_mul_u32_u24 v226, 171, v223\n"
    "v_lshrrev_b32 v226, 10, v226\n"
    "v_mul_u32_u24 v235, 1056, v226\n"
    "v_mul_u32_u24 v240, 288, v226\n"
    "v_add_u32 v235, v235, v250\n"
    "v_add_u32 v240, v240, v250\n"
    "v_add_u32 v223, 64, v232\n"
    "v_mul_u32_u24 v226, 171, v223\n"
    "v_lshrrev_b32 v226, 10, v226\n"
    "v_mul_u32_u24 v238, 1056, v226\n"
    "v_mul_u32_u24 v241, 288, v226\n"
    "v_add_u32 v238, v238, v250\n"
    "v_add_u32 v241, v241, v250\n"
    "v_add_u32 v223, 128, v232\n"
    "v_mul_u32_u24 v226, 171, v223\n"
    "v_lshrrev_b32 v226, 10, v226\n"
    "v_mul_u32_u24 v236, 1056, v226\n"
    "v_mul_u32_u24 v243, 288, v226\n"
    "v_add_u32 v236, v236, v250\n"
    "v_add_u32 v243, v243, v250\n"
    "v_and_b32 v244, 15, v232\n"
    "v_lshrrev_b32 v227, 4, v232\n"
    "v_and_b32 v224, 7, v232\n"
    "v_xor_b32 v223, v244, v227\n"
    "v_lshlrev_b32 v223, 4, v223\n"
    "v_lshl_add_u32 v225, v227, 8, v223\n"
    "v_lshl_add_u32 v230, v244, 8, v223\n"
    "v_lshlrev_b32 v231, 3, v232\n"
    "v_lshlrev_b32 v228, 2, v232\n"
    "s_lshr_b32 s93, %3, 3\n"
    "s_and_b32 s93, s93, 31\n"
    "s_lshl_b32 s93, s93, 2\n"
    "s_add_u32 s93, s93, s26\n"
    "s_lshl_b32 s77, s93, 11\n"
    "s_lshl_b32 s76, s93, 10\n"
    "s_waitcnt lgkmcnt(0)\n"
    "s_and_b32 s49, s49, 0xffff\n"
    "s_mov_b32 s50, 221184\n"
    "s_mov_b32 s51, 0x20000\n"
    "s_and_b32 s71, s71, 0xffff\n"
    "s_mov_b32 s55, 0x20000\n"
    "s_mov_b32 s52, s74\n"
    "s_and_b32 s53, s75, 0xffff\n"
    "s_mov_b32 s54, 16384\n"
    "s_add_u32 m0, s34, 73728\n"
    "s_nop 0\n"
    "buffer_load_dwordx4 v225, s[52:55], s34 offen offset:0 lds\n"
    "v_add_u32 v230, 73728, v230\n"
    "s_lshl_b32 s93, s26, 10\n"
    "s_add_u32 s27, s93, 90112\n"
    "v_add_u32 v229, s27, v228\n"
    "s_mov_b32 s82, 0x3e553b95\n"
    "s_mov_b32 s83, 0x3fb8aa3b\n"
    "v_lshrrev_b32 v223, 2, v232\n"
    "v_and_b32 v226, 3, v232\n"
    "v_mul_u32_u24 v222, 96, v223\n"
    "v_lshl_add_u32 v244, v226, 4, v222\n"
    "v_lshl_add_u32 v222, v226, 3, v222\n"
    "v_add_u32 v239, 64, v222\n"
    "v_mul_u32_u24 v223, 384, v227\n"
    "v_add_u32 v242, v222, v223\n"
    "s_and_b32 s95, s26, 1\n"
    "s_lshl_b32 s35, s95, 5\n"
    "s_add_u32 s93, s87, 65536\n"
    "v_add_u32 v234, s87, v242\n"
    "v_xor_b32 v225, 64, v225\n"
    "buffer_load_dwordx4 v225, s[52:55], s34 offen offset:1024 lds\n"
    "v_add_u32 v233, s93, v242\n"
    "v_add_u32 v242, s93, v244\n"
    "v_add_u32 v244, s93, v239\n"
    "v_add_u32 v237, s35, v233\n"
    "v_subrev_u32 v239, s35, v233\n"
    "v_subrev_u32 v239, s35, v239\n"
    "v_bfe_u32 v223, v232, 3, 1\n"
    "v_mul_u32_u24 v226, 3072, v223\n"
    "v_mul_u32_u24 v222, 384, v224\n"
    "v_add_u32 v226, v226, v222\n"
    "v_xor_b32 v223, v227, v224\n"
    "v_lshl_add_u32 v254, v223, 4, v226\n"
    "v_xor_b32 v248, 64, v254\n"
    "s_mul_i32 s93, s26, 3\n"
    "s_add_u32 s93, s93, 1\n"
    "s_lshr_b32 s93, s93, 1\n"
    "s_lshl_b32 s93, s93, 2\n"
    "v_add_u32 v223, s93, v227\n"
    "v_xor_b32 v225, 192, v225\n"
    "buffer_load_dwordx4 v225, s[52:55], s34 offen offset:2048 lds\n"
    "v_xor_b32 v223, v223, v224\n"
    "v_lshl_add_u32 v223, v223, 4, v226\n"
    "v_add_u32 v246, 98304, v223\n"
    "s_lshr_b32 s93, s26, 1\n"
    "s_mul_i32 s93, s93, 12\n"
    "s_add_u32 s93, s93, 4\n"
    "s_lshl_b32 s94, s95, 3\n"
    "s_add_u32 s94, s94, 98304\n"
    "v_add_u32 v223, s93, v227\n"
    "v_xor_b32 v223, v223, v224\n"
    "v_lshl_add_u32 v223, v223, 4, v226\n"
    "v_add_u32 v252, s94, v223\n"
    "s_mul_i32 s93, s26, 6\n"
    "v_add_u32 v223, s93, v227\n"
    "v_xor_b32 v223, v223, v224\n"
    "v_lshl_add_u32 v245, v223, 4, v226\n"
    "s_add_u32 s93, s93, 4\n"
    "v_lshrrev_b32 v222, 1, v227\n"
    "v_xor_b32 v225, 64, v225\n"
    "buffer_load_dwordx4 v225, s[52:55], s34 offen offset:3072 lds\n"
    "v_add_u32 v223, s93, v222\n"
    "v_xor_b32 v223, v223, v224\n"
    "v_lshl_add_u32 v223, v223, 4, v226\n"
    "v_and_b32 v222, 1, v227\n"
    "v_lshl_add_u32 v253, v222, 3, v223\n"
    "v_mov_b32 v120, 0x3f803f80\n"
    "v_mov_b32 v121, 0x3f803f80\n"
    "v_mov_b32 v122, 0x3f803f80\n"
    "v_mov_b32 v123, 0x3f803f80\n"
    "v_mov_b32 v136, 0\n"
    "v_mov_b32 v137, 0\n"
    "v_mov_b32 v142, 0\n"
    "v_mov_b32 v143, 0\n"
    "v_mov_b32 v255, 0\n"
    "s_and_b32 s41, s41, 0xffff\n"
    "s_mov_b32 s42, 56623104\n"
    "s_mov_b32 s43, 0x20000\n"
    "s_mov_b32 s84, 0\n"
    "s_mov_b32 s52, s72\n"
    "s_and_b32 s53, s73, 0xffff\n"
    "s_mov_b32 s54, 1800\n"
    "s_lshl_b32 s21, s26, 1\n"
    "buffer_load_ushort v108, v231, s[52:55], s21 offen offset:0\n"
    "s_mov_b32 s89, 0\n"
    "s_mov_b32 s91, 98304\n"
    "s_mov_b32 s92, 147456\n"
    "s_mov_b32 s58, 0x80000000\n"
    "s_add_u32 s85, s85, 256\n"
    "s_mul_hi_u32 s28, s85, 91382283\n"
    "s_mul_i32 s29, s28, 47\n"
    "s_sub_u32 s29, s85, s29\n"
    "s_mul_i32 s62, s28, 1179648\n"
    "s_mul_i32 s30, s29, 3072\n"
    "s_add_u32 s62, s62, s30\n"
    "s_add_u32 s62, s62, 591360\n"
    "s_mul_i32 s63, s28, 48\n"
    "s_add_u32 s63, s63, s29\n"
    "s_lshl_b32 s63, s63, 13\n"
    "s_add_u32 s63, s63, s88\n"
    "buffer_load_ushort v109, v231, s[52:55], s21 offen offset:512\n"
    "buffer_load_ushort v110, v231, s[52:55], s21 offen offset:1024\n"
    "buffer_load_ushort v111, v231, s[52:55], s21 offen offset:1536\n"
    "buffer_load_dwordx4 v[96:99], v250, s[48:51], s77 offen offset:0\n"
    "buffer_load_dwordx4 v[104:107], v250, s[48:51], s77 offen offset:1024\n"
    "s_mov_b64 s[52:53], s[70:71]\n"
    "s_mov_b32 s54, 73728\n"
    "buffer_load_dwordx4 v[100:103], v250, s[52:55], s76 offen\n"
    "s_getpc_b64 s[52:53]\n"
    ".Lswin_PC_%=:\n"
    "s_and_b32 s53, s53, 0xffff\n"
    "s_mov_b32 s54, .Lswin_CEND_%=-.Lswin_PC_%=\n"
    "s_lshr_b32 s76, s76, 2\n"
    "buffer_load_dword v232, v228, s[52:55], s76 offen\n"
    "s_and_b32 s37, s37, 0xffff\n"
    "s_mov_b32 s38, 56623104\n"
    "s_mov_b32 s39, 0x20000\n"
    "s_add_u32 s20, s60, s86\n"
    "s_add_u32 m0, s87, 0\n"
    "s_nop 0\n"
    "buffer_load_dwordx4 v251, s[36:39], s20 offen offset:0 lds\n"
    "buffer_load_dwordx4 v249, s[36:39], s20 offen offset:1024 lds\n"
    "buffer_load_dwordx4 v247, s[36:39], s20 offen offset:2048 lds\n"
    "s_add_u32 s20, s60, s86\n"
    "s_add_u32 s20, s20, 147456\n"
    "s_add_u32 m0, s87, 3072\n"
    "s_nop 0\n"
    "buffer_load_dwordx4 v251, s[36:39], s20 offen offset:0 lds\n"
    "buffer_load_dwordx4 v249, s[36:39], s20 offen offset:1024 lds\n"
    "buffer_load_dwordx4 v247, s[36:39], s20 offen offset:2048 lds\n"
    "s_and_b32 s45, s45, 0xffff\n"
    "s_mov_b32 s46, 18874368\n"
    "s_mov_b32 s47, 0x20000\n"
    "s_add_u32 m0, s88, 147456\n"
    "s_nop 0\n"
    "buffer_load_dwordx4 v250, s[44:47], s61 offen offset:0 lds\n"
    "s_waitcnt vmcnt(11)\n"
    "s_barrier\n"
    "ds_read_b128 v[0:3], v230 offset:0\n"
    "ds_read_b128 v[16:19], v230 offset:4096\n"
    "ds_read_b128 v[32:35], v230 offset:8192\n"
    "ds_read_b128 v[48:51], v230 offset:12288\n"
    "v_lshlrev_b32 v108, 16, v108\n"
    "v_mul_f32 v108, s83, v108\n"
    "ds_write_b32 v229, v108 offset:0\n"
    "v_xor_b32 v230, 64, v230\n"
    "ds_read_b128 v[4:7], v230 offset:0\n"
    "buffer_load_dwordx4 v250, s[44:47], s61 offen offset:1024 lds\n"
    "ds_read_b128 v[20:23], v230 offset:4096\n"
    "ds_read_b128 v[36:39], v230 offset:8192\n"
    "ds_read_b128 v[52:55], v230 offset:12288\n"
    "v_lshlrev_b32 v109, 16, v109\n"
    "v_mul_f32 v109, s83, v109\n"
    "ds_write_b32 v229, v109 offset:256\n"
    "v_xor_b32 v230, 192, v230\n"
    "ds_read_b128 v[8:11], v230 offset:0\n"
    "ds_read_b128 v[24:27], v230 offset:4096\n"
    "ds_read_b128 v[40:43], v230 offset:8192\n"
    "s_add_u32 m0, s87, 122880\n"
    "s_add_u32 s21, s31, 384\n"
    "buffer_load_dwordx4 v235, s[48:51], s21 offen offset:0 lds\n"
    "ds_read_b128 v[56:59], v230 offset:12288\n"
    "v_lshlrev_b32 v110, 16, v110\n"
    "v_mul_f32 v110, s83, v110\n"
    "ds_write_b32 v229, v110 offset:512\n"
    "v_xor_b32 v230, 64, v230\n"
    "ds_read_b128 v[12:15], v230 offset:0\n"
    "ds_read_b128 v[28:31], v230 offset:4096\n"
    "ds_read_b128 v[44:47], v230 offset:8192\n"
    "ds_read_b128 v[60:63], v230 offset:12288\n"
    "v_lshlrev_b32 v111, 16, v111\n"
    "v_mul_f32 v111, s83, v111\n"
    "buffer_load_dwordx4 v238, s[48:51], s21 offen offset:1024 lds\n"
    "ds_write_b32 v229, v111 offset:768\n"
    "s_waitcnt lgkmcnt(15)\n"
    "v_lshl_add_u32 v0, v0, 2, s27\n"
    "v_lshl_add_u32 v1, v1, 2, s27\n"
    "v_lshl_add_u32 v2, v2, 2, s27\n"
    "v_lshl_add_u32 v3, v3, 2, s27\n"
    "ds_read_b32 v0, v0\n"
    "ds_read_b32 v1, v1\n"
    "ds_read_b32 v2, v2\n"
    "ds_read_b32 v3, v3\n"
    "v_lshl_add_u32 v16, v16, 2, s27\n"
    "v_lshl_add_u32 v17, v17, 2, s27\n"
    "v_lshl_add_u32 v18, v18, 2, s27\n"
    "buffer_load_dwordx4 v236, s[48:51], s21 offen offset:2048 lds\n"
    "v_lshl_add_u32 v19, v19, 2, s27\n"
    "ds_read_b32 v16, v16\n"
    "ds_read_b32 v17, v17\n"
    "ds_read_b32 v18, v18\n"
    "ds_read_b32 v19, v19\n"
    "v_lshl_add_u32 v32, v32, 2, s27\n"
    "v_lshl_add_u32 v33, v33, 2, s27\n"
    "v_lshl_add_u32 v34, v34, 2, s27\n"
    "v_lshl_add_u32 v35, v35, 2, s27\n"
    "ds_read_b32 v32, v32\n"
    "ds_read_b32 v33, v33\n"
    "ds_read_b32 v34, v34\n"
    "s_add_u32 m0, s87, 125952\n"
    "s_add_u32 s21, s31, 37248\n"
    "buffer_load_dwordx4 v235, s[48:51], s21 offen offset:0 lds\n"
    "ds_read_b32 v35, v35\n"
    "v_lshl_add_u32 v48, v48, 2, s27\n"
    "v_lshl_add_u32 v49, v49, 2, s27\n"
    "v_lshl_add_u32 v50, v50, 2, s27\n"
    "v_lshl_add_u32 v51, v51, 2, s27\n"
    "ds_read_b32 v48, v48\n"
    "ds_read_b32 v49, v49\n"
    "ds_read_b32 v50, v50\n"
    "ds_read_b32 v51, v51\n"
    "s_waitcnt lgkmcnt(15)\n"
    "v_lshl_add_u32 v4, v4, 2, s27\n"
    "v_lshl_add_u32 v5, v5, 2, s27\n"
    "v_lshl_add_u32 v6, v6, 2, s27\n"
    "v_lshl_add_u32 v7, v7, 2, s27\n"
    "buffer_load_dwordx4 v238, s[48:51], s21 offen offset:1024 lds\n"
    "ds_read_b32 v4, v4\n"
    "ds_read_b32 v5, v5\n"
    "ds_read_b32 v6, v6\n"
    "ds_read_b32 v7, v7\n"
    "v_lshl_add_u32 v20, v20, 2, s27\n"
    "v_lshl_add_u32 v21, v21, 2, s27\n"
    "v_lshl_add_u32 v22, v22, 2, s27\n"
    "v_lshl_add_u32 v23, v23, 2, s27\n"
    "ds_read_b32 v20, v20\n"
    "ds_read_b32 v21, v21\n"
    "ds_read_b32 v22, v22\n"
    "buffer_load_dwordx4 v236, s[48:51], s21 offen offset:2048 lds\n"
    "ds_read_b32 v23, v23\n"
    "v_lshl_add_u32 v36, v36, 2, s27\n"
    "v_lshl_add_u32 v37, v37, 2, s27\n"
    "v_lshl_add_u32 v38, v38, 2, s27\n"
    "v_lshl_add_u32 v39, v39, 2, s27\n"
    "ds_read_b32 v36, v36\n"
    "ds_read_b32 v37, v37\n"
    "ds_read_b32 v38, v38\n"
    "ds_read_b32 v39, v39\n"
    "v_lshl_add_u32 v52, v52, 2, s27\n"
    "v_lshl_add_u32 v53, v53, 2, s27\n"
    "v_lshl_add_u32 v54, v54, 2, s27\n"
    "v_lshl_add_u32 v55, v55, 2, s27\n"
    "s_add_u32 m0, s87, 98304\n"
    "s_add_u32 s21, s31, 74112\n"
    "buffer_load_dwordx4 v235, s[48:51], s21 offen offset:0 lds\n"
    "ds_read_b32 v52, v52\n"
    "ds_read_b32 v53, v53\n"
    "ds_read_b32 v54, v54\n"
    "ds_read_b32 v55, v55\n"
    "v_lshl_add_u32 v8, v8, 2, s27\n"
    "v_lshl_add_u32 v9, v9, 2, s27\n"
    "v_lshl_add_u32 v10, v10, 2, s27\n"
    "v_lshl_add_u32 v11, v11, 2, s27\n"
    "ds_read_b32 v8, v8\n"
    "ds_read_b32 v9, v9\n"
    "ds_read_b32 v10, v10\n"
    "buffer_load_dwordx4 v238, s[48:51], s21 offen offset:1024 lds\n"
    "ds_read_b32 v11, v11\n"
    "v_lshl_add_u32 v24, v24, 2, s27\n"
    "v_lshl_add_u32 v25, v25, 2, s27\n"
    "v_lshl_add_u32 v26, v26, 2, s27\n"
    "v_lshl_add_u32 v27, v27, 2, s27\n"
    "ds_read_b32 v24, v24\n"
    "ds_read_b32 v25, v25\n"
    "ds_read_b32 v26, v26\n"
    "ds_read_b32 v27, v27\n"
    "v_lshl_add_u32 v40, v40, 2, s27\n"
    "v_lshl_add_u32 v41, v41, 2, s27\n"
    "v_lshl_add_u32 v42, v42, 2, s27\n"
    "v_lshl_add_u32 v43, v43, 2, s27\n"
    "buffer_load_dwordx4 v236, s[48:51], s21 offen offset:2048 lds\n"
    "ds_read_b32 v40, v40\n"
    "ds_read_b32 v41, v41\n"
    "ds_read_b32 v42, v42\n"
    "ds_read_b32 v43, v43\n"
    "v_lshl_add_u32 v56, v56, 2, s27\n"
    "v_lshl_add_u32 v57, v57, 2, s27\n"
    "v_lshl_add_u32 v58, v58, 2, s27\n"
    "v_lshl_add_u32 v59, v59, 2, s27\n"
    "ds_read_b32 v56, v56\n"
    "ds_read_b32 v57, v57\n"
    "ds_read_b32 v58, v58\n"
    "s_add_u32 m0, s87, 101376\n"
    "s_add_u32 s21, s31, 110976\n"
    "buffer_load_dwordx4 v235, s[48:51], s21 offen offset:0 lds\n"
    "ds_read_b32 v59, v59\n"
    "v_lshl_add_u32 v12, v12, 2, s27\n"
    "v_lshl_add_u32 v13, v13, 2, s27\n"
    "v_lshl_add_u32 v14, v14, 2, s27\n"
    "v_lshl_add_u32 v15, v15, 2, s27\n"
    "ds_read_b32 v12, v12\n"
    "ds_read_b32 v13, v13\n"
    "ds_read_b32 v14, v14\n"
    "ds_read_b32 v15, v15\n"
    "v_lshl_add_u32 v28, v28, 2, s27\n"
    "v_lshl_add_u32 v29, v29, 2, s27\n"
    "v_lshl_add_u32 v30, v30, 2, s27\n"
    "v_lshl_add_u32 v31, v31, 2, s27\n"
    "buffer_load_dwordx4 v238, s[48:51], s21 offen offset:1024 lds\n"
    "ds_read_b32 v28, v28\n"
    "ds_read_b32 v29, v29\n"
    "ds_read_b32 v30, v30\n"
    "ds_read_b32 v31, v31\n"
    "v_lshl_add_u32 v44, v44, 2, s27\n"
    "v_lshl_add_u32 v45, v45, 2, s27\n"
    "v_lshl_add_u32 v46, v46, 2, s27\n"
    "v_lshl_add_u32 v47, v47, 2, s27\n"
    "ds_read_b32 v44, v44\n"
    "ds_read_b32 v45, v45\n"
    "ds_read_b32 v46, v46\n"
    "buffer_load_dwordx4 v236, s[48:51], s21 offen offset:2048 lds\n"
    "ds_read_b32 v47, v47\n"
    "v_lshl_add_u32 v60, v60, 2, s27\n"
    "v_lshl_add_u32 v61, v61, 2, s27\n"
    "v_lshl_add_u32 v62, v62, 2, s27\n"
    "v_lshl_add_u32 v63, v63, 2, s27\n"
    "ds_read_b32 v60, v60\n"
    "ds_read_b32 v61, v61\n"
    "ds_read_b32 v62, v62\n"
    "ds_read_b32 v63, v63\n"
    "s_waitcnt vmcnt(14)\n"
    "s_barrier\n"
    "ds_read_b128 a[216:219], v254 offset:0\n"
    "s_add_u32 m0, s87, 49152\n"
    "s_add_u32 s21, s31, 147840\n"
    "buffer_load_dwordx4 v235, s[48:51], s21 offen offset:0 lds\n"
    "ds_read_b128 a[240:243], v248 offset:0\n"
    "ds_read_b128 v[72:75], v254 offset:128\n"
    "ds_read_b128 a[220:223], v248 offset:128\n"
    "ds_read_b128 a[244:247], v254 offset:256\n"
    "ds_read_b128 v[76:79], v248 offset:256\n"
    "ds_read_b128 a[224:227], v254 offset:6144\n"
    "ds_read_b128 a[248:251], v248 offset:6144\n"
    "ds_read_b128 v[80:83], v254 offset:6272\n"
    "ds_read_b128 a[228:231], v248 offset:6272\n"
    "buffer_load_dwordx4 v238, s[48:51], s21 offen offset:1024 lds\n"
    "ds_read_b128 a[252:255], v254 offset:6400\n"
    "ds_read_b128 v[84:87], v248 offset:6400\n"
    "ds_read_b128 a[232:235], v254 offset:12288\n"
    "ds_read_b128 v[64:67], v248 offset:12288\n"
    "ds_read_b128 v[88:91], v254 offset:12416\n"
    "ds_read_b128 a[236:239], v248 offset:12416\n"
    "ds_read_b128 v[68:71], v254 offset:12544\n"
    "ds_read_b128 v[92:95], v248 offset:12544\n"
    "ds_read_b128 v[198:201], v254 offset:18432\n"
    "buffer_load_dwordx4 v236, s[48:51], s21 offen offset:2048 lds\n"
    "ds_read_b128 v[128:131], v248 offset:18432\n"
    "ds_read_b128 v[124:127], v254 offset:18560\n"
    "ds_read_b128 v[186:189], v248 offset:18560\n"
    "ds_read_b128 v[190:193], v254 offset:18688\n"
    "ds_read_b128 v[194:197], v248 offset:18688\n"
    "s_waitcnt vmcnt(12)\n"
    "ds_read_b64_tr_b16 a[72:73], v233 offset:57344\n"
    "ds_read_b64_tr_b16 a[74:75], v233 offset:57728\n"
    "ds_read_b64_tr_b16 a[96:97], v233 offset:57376\n"
    "ds_read_b64_tr_b16 a[98:99], v233 offset:57760\n"
    "s_add_u32 m0, s87, 52224\n"
    "s_add_u32 s21, s31, 184704\n"
    "buffer_load_dwordx4 v235, s[48:51], s21 offen offset:0 lds\n"
    "ds_read_b64_tr_b16 a[120:121], v233 offset:57408\n"
    "ds_read_b64_tr_b16 a[122:123], v233 offset:57792\n"
    "buffer_load_dwordx4 v238, s[48:51], s21 offen offset:1024 lds\n"
    "s_waitcnt vmcnt(11)\n"
    "ds_read_b64_tr_b16 a[76:77], v233 offset:60416\n"
    "ds_read_b64_tr_b16 a[78:79], v233 offset:60800\n"
    "ds_read_b64_tr_b16 a[100:101], v233 offset:60448\n"
    "ds_read_b64_tr_b16 a[102:103], v233 offset:60832\n"
    "ds_read_b64_tr_b16 a[124:125], v233 offset:60480\n"
    "ds_read_b64_tr_b16 a[126:127], v233 offset:60864\n"
    "buffer_load_dwordx4 v236, s[48:51], s21 offen offset:2048 lds\n"
    "s_add_u32 m0, s87, 24576\n"
    "s_add_u32 s21, s31, 0\n"
    "buffer_load_dwordx4 v235, s[48:51], s21 offen offset:0 lds\n"
    "buffer_load_dwordx4 v238, s[48:51], s21 offen offset:1024 lds\n"
    "s_waitcnt vmcnt(11)\n"
    "ds_read_b64_tr_b16 a[80:81], v233 offset:32768\n"
    "ds_read_b64_tr_b16 a[82:83], v233 offset:33152\n"
    "ds_read_b64_tr_b16 a[104:105], v233 offset:32800\n"
    "ds_read_b64_tr_b16 a[106:107], v233 offset:33184\n"
    "ds_read_b64_tr_b16 a[128:129], v233 offset:32832\n"
    "ds_read_b64_tr_b16 a[130:131], v233 offset:33216\n"
    "buffer_load_dwordx4 v236, s[48:51], s21 offen offset:2048 lds\n"
    "s_add_u32 m0, s87, 27648\n"
    "s_add_u32 s21, s31, 36864\n"
    "buffer_load_dwordx4 v235, s[48:51], s21 offen offset:0 lds\n"
    "buffer_load_dwordx4 v238, s[48:51], s21 offen offset:1024 lds\n"
    "s_waitcnt vmcnt(11)\n"
    "ds_read_b64_tr_b16 a[84:85], v233 offset:35840\n"
    "ds_read_b64_tr_b16 a[86:87], v233 offset:36224\n"
    "ds_read_b64_tr_b16 a[108:109], v233 offset:35872\n"
    "ds_read_b64_tr_b16 a[110:111], v233 offset:36256\n"
    "ds_read_b64_tr_b16 a[132:133], v233 offset:35904\n"
    "ds_read_b64_tr_b16 a[134:135], v233 offset:36288\n"
    "buffer_load_dwordx4 v236, s[48:51], s21 offen offset:2048 lds\n"
    "s_waitcnt vmcnt(9)\n"
    "ds_read_b64_tr_b16 a[88:89], v234 offset:49152\n"
    "ds_read_b64_tr_b16 a[90:91], v234 offset:49536\n"
    "ds_read_b64_tr_b16 a[112:113], v234 offset:49184\n"
    "ds_read_b64_tr_b16 a[114:115], v234 offset:49568\n"
    "ds_read_b64_tr_b16 a[136:137], v234 offset:49216\n"
    "ds_read_b64_tr_b16 a[138:139], v234 offset:49600\n"
    "s_waitcnt vmcnt(6)\n"
    "ds_read_b64_tr_b16 a[92:93], v234 offset:52224\n"
    "ds_read_b64_tr_b16 a[94:95], v234 offset:52608\n"
    "ds_read_b64_tr_b16 a[116:117], v234 offset:52256\n"
    "ds_read_b64_tr_b16 a[118:119], v234 offset:52640\n"
    "ds_read_b64_tr_b16 a[140:141], v234 offset:52288\n"
    "ds_read_b64_tr_b16 a[142:143], v234 offset:52672\n"
    "s_waitcnt lgkmcnt(0)\n"
    "v_mfma_f32_16x16x32_bf16 v[108:111], a[72:75], a[216:219], 0\n"
    "s_barrier\n"
    "v_add_u32 v233, s92, v250\n"
    "s_add_u32 m0, s87, 49152\n"
    "v_mfma_f32_16x16x32_bf16 v[108:111], a[76:79], a[240:243], v[108:111]\n"
    "s_add_u32 s21, s31, 73728\n"
    "s_add_u32 s85, s85, 256\n"
    "s_mul_hi_u32 s28, s85, 91382283\n"
    "v_mfma_f32_16x16x32_bf16 v[108:111], a[80:83], v[72:75], v[108:111]\n"
    "buffer_load_dwordx4 v235, s[48:51], s21 offen offset:0 lds\n"
    "v_mfma_f32_16x16x32_bf16 v[108:111], a[84:87], a[220:223], v[108:111]\n"
    "s_mul_i32 s29, s28, 47\n"
    "s_sub_u32 s29, s85, s29\n"
    "s_mul_i32 s64, s28, 1179648\n"
    "v_mfma_f32_16x16x32_bf16 v[108:111], a[88:91], a[244:247], v[108:111]\n"
    "s_waitcnt vmcnt(4)\n"
    "ds_read_b64_tr_b16 a[0:1], v234 offset:24576\n"
    "v_mfma_f32_16x16x32_bf16 v[108:111], a[92:95], v[76:79], v[108:111]\n"
    "buffer_load_dwordx4 v238, s[48:51], s21 offen offset:1024 lds\n"
    "v_mfma_f32_16x16x32_bf16 v[96:99], a[96:99], a[216:219], 0\n"
    "s_mul_i32 s30, s29, 3072\n"
    "ds_read_b64_tr_b16 a[2:3], v234 offset:24960\n"
    "v_mfma_f32_16x16x32_bf16 v[96:99], a[100:103], a[240:243], v[96:99]\n"
    "s_add_u32 s64, s64, s30\n"
    "ds_read_b64_tr_b16 a[24:25], v234 offset:24608\n"
    "v_mfma_f32_16x16x32_bf16 v[96:99], a[104:107], v[72:75], v[96:99]\n"
    "buffer_load_dwordx4 v236, s[48:51], s21 offen offset:2048 lds\n"
    "v_mfma_f32_16x16x32_bf16 v[96:99], a[108:111], a[220:223], v[96:99]\n"
    "s_add_u32 m0, s87, 52224\n"
    "ds_read_b64_tr_b16 a[26:27], v234 offset:24992\n"
    "v_mfma_f32_16x16x32_bf16 v[96:99], a[112:115], a[244:247], v[96:99]\n"
    "s_add_u32 s21, s31, 110592\n"
    "ds_read_b64_tr_b16 a[48:49], v234 offset:24640\n"
    "v_mfma_f32_16x16x32_bf16 v[96:99], a[116:119], v[76:79], v[96:99]\n"
    "buffer_load_dwordx4 v235, s[48:51], s21 offen offset:0 lds\n"
    "v_mfma_f32_16x16x32_bf16 v[100:103], a[120:123], a[216:219], 0\n"
    "s_waitcnt vmcnt(4)\n"
    "ds_read_b64_tr_b16 a[50:51], v234 offset:25024\n"
    "v_mfma_f32_16x16x32_bf16 v[100:103], a[124:127], a[240:243], v[100:103]\n"
    "s_add_u32 s64, s64, 591360\n"
    "v_cvt_pk_bf16_f32 v158, v108, v109\n"
    "v_cvt_pk_bf16_f32 v159, v110, v111\n"
    "v_mfma_f32_16x16x32_bf16 v[100:103], a[128:131], v[72:75], v[100:103]\n"
    "buffer_load_dwordx4 v238, s[48:51], s21 offen offset:1024 lds\n"
    "v_mfma_f32_16x16x32_bf16 v[100:103], a[132:135], a[220:223], v[100:103]\n"
    "ds_read_b64_tr_b16 a[4:5], v234 offset:27648\n"
    "v_cvt_pk_bf16_f32 v160, v96, v97\n"
    "v_mfma_f32_16x16x32_bf16 v[100:103], a[136:139], a[244:247], v[100:103]\n"
    "ds_read_b64_tr_b16 a[6:7], v234 offset:28032\n"
    "v_cvt_pk_bf16_f32 v161, v98, v99\n"
    "v_mfma_f32_16x16x32_bf16 v[100:103], a[140:143], v[76:79], v[100:103]\n"
    "buffer_load_dwordx4 v236, s[48:51], s21 offen offset:2048 lds\n"
    "v_mfma_f32_16x16x32_bf16 v[104:107], a[72:75], a[224:227], 0\n"
    "s_add_u32 m0, s87, 0\n"
    "ds_read_b64_tr_b16 a[28:29], v234 offset:27680\n"
    "v_mfma_f32_16x16x32_bf16 v[104:107], a[76:79], a[248:251], v[104:107]\n"
    "s_add_u32 s21, s31, 147456\n"
    "ds_read_b64_tr_b16 a[30:31], v234 offset:28064\n"
    "v_mfma_f32_16x16x32_bf16 v[104:107], a[80:83], v[80:83], v[104:107]\n"
    "buffer_load_dwordx4 v235, s[48:51], s21 offen offset:0 lds\n"
    "v_mfma_f32_16x16x32_bf16 v[104:107], a[84:87], a[228:231], v[104:107]\n"
    "ds_read_b64_tr_b16 a[52:53], v234 offset:27712\n"
    "v_cvt_pk_bf16_f32 v134, v100, v101\n"
    "v_mfma_f32_16x16x32_bf16 v[104:107], a[88:91], a[252:255], v[104:107]\n"
    "ds_read_b64_tr_b16 a[54:55], v234 offset:28096\n"
    "v_cvt_pk_bf16_f32 v135, v102, v103\n"
    "v_mfma_f32_16x16x32_bf16 v[104:107], a[92:95], v[84:87], v[104:107]\n"
    "buffer_load_dwordx4 v238, s[48:51], s21 offen offset:1024 lds\n"
    "v_mfma_f32_16x16x32_bf16 v[96:99], a[96:99], a[224:227], 0\n"
    "s_mul_i32 s65, s28, 48\n"
    "s_add_u32 s65, s65, s29\n"
    "s_lshl_b32 s65, s65, 13\n"
    "v_mfma_f32_16x16x32_bf16 v[96:99], a[100:103], a[248:251], v[96:99]\n"
    "s_add_u32 s65, s65, s88\n"
    "v_cvt_pk_bf16_f32 v162, v104, v105\n"
    "v_cvt_pk_bf16_f32 v163, v106, v107\n"
    "v_mfma_f32_16x16x32_bf16 v[96:99], a[104:107], v[80:83], v[96:99]\n"
    "buffer_load_dwordx4 v236, s[48:51], s21 offen offset:2048 lds\n"
    "v_mfma_f32_16x16x32_bf16 v[96:99], a[108:111], a[228:231], v[96:99]\n"
    "s_add_u32 m0, s87, 3072\n"
    "s_add_u32 s21, s31, 184320\n"
    "s_add_u32 s98, s87, 73728\n"
    "v_mfma_f32_16x16x32_bf16 v[96:99], a[112:115], a[252:255], v[96:99]\n"
    "s_add_u32 s20, s62, s86\n"
    "s_mov_b32 s90, s89\n"
    "s_sub_u32 s23, 221184, s91\n"
    "v_mfma_f32_16x16x32_bf16 v[96:99], a[116:119], v[84:87], v[96:99]\n"
    "buffer_load_dwordx4 v235, s[48:51], s21 offen offset:0 lds\n"
    "v_mfma_f32_16x16x32_bf16 v[104:107], a[120:123], a[224:227], 0\n"
    "s_sub_u32 s23, s23, s89\n"
    "v_add_u32 v254, s23, v254\n"
    "v_add_u32 v248, s23, v248\n"
    "v_mfma_f32_16x16x32_bf16 v[104:107], a[124:127], a[248:251], v[104:107]\n"
    "s_add_u32 s89, s89, 24576\n"
    "v_cvt_pk_bf16_f32 v164, v96, v97\n"
    "v_cvt_pk_bf16_f32 v165, v98, v99\n"
    "v_mfma_f32_16x16x32_bf16 v[104:107], a[128:131], v[80:83], v[104:107]\n"
    "buffer_load_dwordx4 v238, s[48:51], s21 offen offset:1024 lds\n"
    "v_mfma_f32_16x16x32_bf16 v[104:107], a[132:135], a[228:231], v[104:107]\n"
    "s_cmp_eq_u32 s89, 98304\n"
    "s_cselect_b32 s89, 0, s89\n"
    "s_add_u32 s95, s89, s91\n"
    "v_mfma_f32_16x16x32_bf16 v[104:107], a[136:139], a[252:255], v[104:107]\n"
    "s_sub_u32 s95, s95, 221184\n"
    "v_add_u32 v254, s95, v254\n"
    "v_add_u32 v248, s95, v248\n"
    "v_mfma_f32_16x16x32_bf16 v[104:107], a[140:143], v[84:87], v[104:107]\n"
    "buffer_load_dwordx4 v236, s[48:51], s21 offen offset:2048 lds\n"
    "v_mfma_f32_16x16x32_bf16 v[100:103], a[72:75], a[232:235], 0\n"
    "s_add_u32 m0, s87, 73728\n"
    "s_add_u32 s21, s31, 768\n"
    "s_mov_b64 s[56:57], s[58:59]\n"
    "v_mfma_f32_16x16x32_bf16 v[100:103], a[76:79], v[64:67], v[100:103]\n"
    "s_add_u32 s85, s85, 256\n"
    "v_cvt_pk_bf16_f32 v138, v104, v105\n"
    "v_cvt_pk_bf16_f32 v139, v106, v107\n"
    "v_mfma_f32_16x16x32_bf16 v[100:103], a[80:83], v[88:91], v[100:103]\n"
    "buffer_load_dwordx4 v235, s[48:51], s21 offen offset:0 lds\n"
    "v_mfma_f32_16x16x32_bf16 v[100:103], a[84:87], a[236:239], v[100:103]\n"
    "s_mul_hi_u32 s28, s85, 91382283\n"
    "s_waitcnt vmcnt(10)\n"
    "s_mul_i32 s29, s28, 47\n"
    "v_mfma_f32_16x16x32_bf16 v[100:103], a[88:91], v[68:71], v[100:103]\n"
    "s_sub_u32 s29, s85, s29\n"
    "ds_read_b64_tr_b16 a[8:9], v234 offset:49152\n"
    "v_mfma_f32_16x16x32_bf16 v[100:103], a[92:95], v[92:95], v[100:103]\n"
    "buffer_load_dwordx4 v238, s[48:51], s21 offen offset:1024 lds\n"
    "v_mfma_f32_16x16x32_bf16 v[96:99], a[96:99], a[232:235], 0\n"
    "s_mul_i32 s30, s29, 3072\n"
    "ds_read_b64_tr_b16 a[10:11], v234 offset:49536\n"
    "v_mfma_f32_16x16x32_bf16 v[96:99], a[100:103], v[64:67], v[96:99]\n"
    "ds_read_b64_tr_b16 a[32:33], v234 offset:49184\n"
    "v_cvt_pk_bf16_f32 v178, v100, v101\n"
    "v_mfma_f32_16x16x32_bf16 v[96:99], a[104:107], v[88:91], v[96:99]\n"
    "buffer_load_dwordx4 v236, s[48:51], s21 offen offset:2048 lds\n"
    "v_mfma_f32_16x16x32_bf16 v[96:99], a[108:111], a[236:239], v[96:99]\n"
    "s_add_u32 m0, s87, 76800\n"
    "ds_read_b64_tr_b16 a[34:35], v234 offset:49568\n"
    "v_mfma_f32_16x16x32_bf16 v[96:99], a[112:115], v[68:71], v[96:99]\n"
    "s_add_u32 s21, s31, 37632\n"
    "ds_read_b64_tr_b16 a[56:57], v234 offset:49216\n"
    "v_mfma_f32_16x16x32_bf16 v[96:99], a[116:119], v[92:95], v[96:99]\n"
    "ds_read_b64_tr_b16 a[58:59], v234 offset:49600\n"
    "v_cvt_pk_bf16_f32 v179, v102, v103\n"
    "v_mfma_f32_16x16x32_bf16 v[100:103], a[120:123], a[232:235], 0\n"
    "buffer_load_dwordx4 v235, s[48:51], s21 offen offset:0 lds\n"
    "v_mfma_f32_16x16x32_bf16 v[100:103], a[124:127], v[64:67], v[100:103]\n"
    "s_waitcnt vmcnt(10)\n"
    "ds_read_b64_tr_b16 a[12:13], v234 offset:52224\n"
    "v_mfma_f32_16x16x32_bf16 v[100:103], a[128:131], v[88:91], v[100:103]\n"
    "ds_read_b64_tr_b16 a[14:15], v234 offset:52608\n"
    "v_cvt_pk_bf16_f32 v180, v96, v97\n"
    "v_mfma_f32_16x16x32_bf16 v[100:103], a[132:135], a[236:239], v[100:103]\n"
    "buffer_load_dwordx4 v238, s[48:51], s21 offen offset:1024 lds\n"
    "v_mfma_f32_16x16x32_bf16 v[100:103], a[136:139], v[68:71], v[100:103]\n"
    "ds_read_b64_tr_b16 a[36:37], v234 offset:52256\n"
    "v_cvt_pk_bf16_f32 v181, v98, v99\n"
    "v_mfma_f32_16x16x32_bf16 v[100:103], a[140:143], v[92:95], v[100:103]\n"
    "ds_read_b64_tr_b16 a[38:39], v234 offset:52640\n"
    "v_mfma_f32_16x16x32_bf16 v[96:99], a[72:75], v[198:201], 0\n"
    "buffer_load_dwordx4 v236, s[48:51], s21 offen offset:2048 lds\n"
    "v_mfma_f32_16x16x32_bf16 v[96:99], a[76:79], v[128:131], v[96:99]\n"
    "s_add_u32 m0, s87, 122880\n"
    "ds_read_b64_tr_b16 a[60:61], v234 offset:52288\n"
    "v_mfma_f32_16x16x32_bf16 v[96:99], a[80:83], v[124:127], v[96:99]\n"
    "s_add_u32 s21, s31, 74496\n"
    "ds_read_b64_tr_b16 a[62:63], v234 offset:52672\n"
    "v_mfma_f32_16x16x32_bf16 v[96:99], a[84:87], v[186:189], v[96:99]\n"
    "buffer_load_dwordx4 v235, s[48:51], s21 offen offset:0 lds\n"
    "v_mfma_f32_16x16x32_bf16 v[96:99], a[88:91], v[190:193], v[96:99]\n"
    "s_waitcnt vmcnt(10)\n"
    "v_cvt_pk_bf16_f32 v140, v100, v101\n"
    "v_cvt_pk_bf16_f32 v141, v102, v103\n"
    "v_mfma_f32_16x16x32_bf16 v[96:99], a[92:95], v[194:197], v[96:99]\n"
    "ds_read_b64_tr_b16 a[16:17], v234 offset:0\n"
    "v_mfma_f32_16x16x32_bf16 v[100:103], a[96:99], v[198:201], 0\n"
    "buffer_load_dwordx4 v238, s[48:51], s21 offen offset:1024 lds\n"
    "v_mfma_f32_16x16x32_bf16 v[100:103], a[100:103], v[128:131], v[100:103]\n"
    "ds_read_b64_tr_b16 a[18:19], v234 offset:384\n"
    "v_cvt_pk_bf16_f32 v182, v96, v97\n"
    "v_mfma_f32_16x16x32_bf16 v[100:103], a[104:107], v[124:127], v[100:103]\n"
    "ds_read_b64_tr_b16 a[40:41], v234 offset:32\n"
    "v_cvt_pk_bf16_f32 v183, v98, v99\n"
    "v_mfma_f32_16x16x32_bf16 v[100:103], a[108:111], v[186:189], v[100:103]\n"
    "buffer_load_dwordx4 v236, s[48:51], s21 offen offset:2048 lds\n"
    "v_mfma_f32_16x16x32_bf16 v[100:103], a[112:115], v[190:193], v[100:103]\n"
    "s_add_u32 m0, s87, 125952\n"
    "ds_read_b64_tr_b16 a[42:43], v234 offset:416\n"
    "v_mfma_f32_16x16x32_bf16 v[100:103], a[116:119], v[194:197], v[100:103]\n"
    "s_add_u32 s21, s31, 111360\n"
    "ds_read_b64_tr_b16 a[64:65], v234 offset:64\n"
    "v_mfma_f32_16x16x32_bf16 v[96:99], a[120:123], v[198:201], 0\n"
    "buffer_load_dwordx4 v235, s[48:51], s21 offen offset:0 lds\n"
    "v_mfma_f32_16x16x32_bf16 v[96:99], a[124:127], v[128:131], v[96:99]\n"
    "s_waitcnt vmcnt(10)\n"
    "ds_read_b64_tr_b16 a[66:67], v234 offset:448\n"
    "v_mfma_f32_16x16x32_bf16 v[96:99], a[128:131], v[124:127], v[96:99]\n"
    "ds_read_b64_tr_b16 a[20:21], v234 offset:3072\n"
    "v_cvt_pk_bf16_f32 v184, v100, v101\n"
    "v_mfma_f32_16x16x32_bf16 v[96:99], a[132:135], v[186:189], v[96:99]\n"
    "buffer_load_dwordx4 v238, s[48:51], s21 offen offset:1024 lds\n"
    "v_mfma_f32_16x16x32_bf16 v[96:99], a[136:139], v[190:193], v[96:99]\n"
    "ds_read_b64_tr_b16 a[22:23], v234 offset:3456\n"
    "v_cvt_pk_bf16_f32 v185, v102, v103\n"
    "v_mfma_f32_16x16x32_bf16 v[96:99], a[140:143], v[194:197], v[96:99]\n"
    "ds_read_b64_tr_b16 a[44:45], v234 offset:3104\n"
    "s_waitcnt lgkmcnt(5)\n"
    "v_mfma_f32_16x16x32_bf16 v[154:157], a[0:3], a[216:219], 0\n"
    "buffer_load_dwordx4 v236, s[48:51], s21 offen offset:2048 lds\n"
    "v_mfma_f32_16x16x32_bf16 v[154:157], a[4:7], a[240:243], v[154:157]\n"
    "s_add_u32 m0, s87, 98304\n"
    "ds_read_b64_tr_b16 a[46:47], v234 offset:3488\n"
    "v_mfma_f32_16x16x32_bf16 v[154:157], a[8:11], v[72:75], v[154:157]\n"
    "s_add_u32 s21, s31, 148224\n"
    "ds_read_b64_tr_b16 a[68:69], v234 offset:3136\n"
    "v_mfma_f32_16x16x32_bf16 v[154:157], a[12:15], a[220:223], v[154:157]\n"
    "ds_read_b64_tr_b16 a[70:71], v234 offset:3520\n"
    "v_cvt_pk_bf16_f32 v144, v96, v97\n"
    "v_mfma_f32_16x16x32_bf16 v[154:157], a[16:19], a[244:247], v[154:157]\n"
    "buffer_load_dwordx4 v235, s[48:51], s21 offen offset:0 lds\n"
    "s_waitcnt lgkmcnt(4)\n"
    "v_mfma_f32_16x16x32_bf16 v[154:157], a[20:23], v[76:79], v[154:157]\n"
    "ds_read_b128 v[150:153], v233 offset:0\n"
    "v_cvt_pk_bf16_f32 v145, v98, v99\n"
    "v_mfma_f32_16x16x32_bf16 v[100:103], a[24:27], a[216:219], 0\n"
    "ds_read_b128 v[112:115], v233 offset:1024\n"
    "v_mfma_f32_16x16x32_bf16 v[100:103], a[28:31], a[240:243], v[100:103]\n"
    "buffer_load_dwordx4 v238, s[48:51], s21 offen offset:1024 lds\n"
    "v_mfma_f32_16x16x32_bf16 v[100:103], a[32:35], v[72:75], v[100:103]\n"
    "ds_read_b128 v[96:99], v233 offset:2048\n"
    "v_mul_f32 v154, s82, v154\n"
    "v_mfma_f32_16x16x32_bf16 v[100:103], a[36:39], a[220:223], v[100:103]\n"
    "ds_read_b128 v[116:119], v233 offset:3072\n"
    "v_mul_f32 v155, s82, v155\n"
    "v_mfma_f32_16x16x32_bf16 v[100:103], a[40:43], a[244:247], v[100:103]\n"
    "s_waitcnt vmcnt(11)\n"
    "ds_read_b128 v[170:173], v233 offset:4096\n"
    "s_waitcnt lgkmcnt(5)\n"
    "v_mfma_f32_16x16x32_bf16 v[100:103], a[44:47], v[76:79], v[100:103]\n"
    "buffer_load_dwordx4 v236, s[48:51], s21 offen offset:2048 lds\n"
    "v_mfma_f32_16x16x32_bf16 v[104:107], a[48:51], a[216:219], 0\n"
    "s_add_u32 m0, s87, 101376\n"
    "ds_read_b128 v[166:169], v233 offset:5120\n"
    "v_mfma_f32_16x16x32_bf16 v[104:107], a[52:55], a[240:243], v[104:107]\n"
    "s_add_u32 s21, s31, 185088\n"
    "ds_read_b128 v[108:111], v233 offset:6144\n"
    "v_mfma_f32_16x16x32_bf16 v[104:107], a[56:59], v[72:75], v[104:107]\n"
    "ds_read_b128 v[146:149], v233 offset:7168\n"
    "s_waitcnt lgkmcnt(6)\n"
    "v_or3_b32 v234, v150, v151, v152\n"
    "v_mfma_f32_16x16x32_bf16 v[104:107], a[60:63], a[220:223], v[104:107]\n"
    "buffer_load_dwordx4 v235, s[48:51], s21 offen offset:0 lds\n"
    "v_mfma_f32_16x16x32_bf16 v[104:107], a[64:67], a[244:247], v[104:107]\n"
    "ds_read_b64_tr_b16 a[144:145], v237 offset:8192\n"
    "v_or_b32 v234, v234, v153\n"
    "v_mfma_f32_16x16x32_bf16 v[104:107], a[68:71], v[76:79], v[104:107]\n"
    "ds_read_b64_tr_b16 a[146:147], v237 offset:8576\n"
    "v_or3_b32 v234, v234, v112, v113\n"
    "v_mfma_f32_16x16x32_bf16 v[150:153], a[0:3], a[224:227], 0\n"
    "buffer_load_dwordx4 v238, s[48:51], s21 offen offset:1024 lds\n"
    "v_mfma_f32_16x16x32_bf16 v[150:153], a[4:7], a[248:251], v[150:153]\n"
    "ds_read_b64_tr_b16 a[168:169], v237 offset:8224\n"
    "v_or3_b32 v234, v234, v114, v115\n"
    "v_mfma_f32_16x16x32_bf16 v[150:153], a[8:11], v[80:83], v[150:153]\n"
    "ds_read_b64_tr_b16 a[170:171], v237 offset:8608\n"
    "s_waitcnt lgkmcnt(5)\n"
    "v_or3_b32 v234, v234, v96, v97\n"
    "v_mfma_f32_16x16x32_bf16 v[150:153], a[12:15], a[228:231], v[150:153]\n"
    "buffer_load_dwordx4 v236, s[48:51], s21 offen offset:2048 lds\n"
    "v_mfma_f32_16x16x32_bf16 v[150:153], a[16:19], a[252:255], v[150:153]\n"
    "ds_read_b64_tr_b16 a[192:193], v239 offset:8256\n"
    "v_or3_b32 v234, v234, v98, v99\n"
    "v_mfma_f32_16x16x32_bf16 v[150:153], a[20:23], v[84:87], v[150:153]\n"
    "ds_read_b64_tr_b16 a[194:195], v239 offset:8640\n"
    "v_mul_f32 v156, s82, v156\n"
    "v_mfma_f32_16x16x32_bf16 v[112:115], a[24:27], a[224:227], 0\n"
    "s_mov_b64 s[48:49], s[70:71]\n"
    "v_mul_f32 v157, s82, v157\n"
    "v_cvt_pk_bf16_f32 v96, v154, v155\n"
    "v_mfma_f32_16x16x32_bf16 v[112:115], a[28:31], a[248:251], v[112:115]\n"
    "s_mov_b32 s50, 73728\n"
    "v_cvt_pk_bf16_f32 v97, v156, v157\n"
    "v_or3_b32 v234, v234, v116, v117\n"
    "v_mfma_f32_16x16x32_bf16 v[112:115], a[32:35], v[80:83], v[112:115]\n"
    "s_add_u32 m0, s87, 73728\n"
    "v_or3_b32 v234, v234, v118, v119\n"
    "v_or3_b32 v234, v234, v170, v171\n"
    "v_mfma_f32_16x16x32_bf16 v[112:115], a[36:39], a[228:231], v[112:115]\n"
    "s_add_u32 s21, s31, 0\n"
    "v_or3_b32 v234, v234, v172, v173\n"
    "v_or3_b32 v234, v234, v166, v167\n"
    "v_mfma_f32_16x16x32_bf16 v[112:115], a[40:43], a[252:255], v[112:115]\n"
    "s_waitcnt vmcnt(12)\n"
    "v_or3_b32 v234, v234, v168, v169\n"
    "v_mul_f32 v100, s82, v100\n"
    "v_mfma_f32_16x16x32_bf16 v[112:115], a[44:47], v[84:87], v[112:115]\n"
    "ds_read_b64_tr_b16 a[148:149], v237 offset:11264\n"
    "v_mul_f32 v101, s82, v101\n"
    "v_mfma_f32_16x16x32_bf16 v[116:119], a[48:51], a[224:227], 0\n"
    "ds_read_b64_tr_b16 a[150:151], v237 offset:11648\n"
    "v_mul_f32 v102, s82, v102\n"
    "v_mfma_f32_16x16x32_bf16 v[116:119], a[52:55], a[248:251], v[116:119]\n"
    "s_waitcnt lgkmcnt(2)\n"
    "buffer_load_dwordx4 v240, s[48:51], s21 offen offset:0 lds\n"
    "v_mfma_f32_16x16x32_bf16 v[116:119], a[56:59], v[80:83], v[116:119]\n"
    "ds_read_b64_tr_b16 a[172:173], v237 offset:11296\n"
    "v_mul_f32 v103, s82, v103\n"
    "v_mfma_f32_16x16x32_bf16 v[116:119], a[60:63], a[228:231], v[116:119]\n"
    "ds_read_b64_tr_b16 a[174:175], v237 offset:11680\n"
    "v_cvt_pk_bf16_f32 v98, v100, v101\n"
    "v_mfma_f32_16x16x32_bf16 v[116:119], a[64:67], a[252:255], v[116:119]\n"
    "buffer_load_dwordx4 v241, s[48:51], s21 offen offset:1024 lds\n"
    "v_mfma_f32_16x16x32_bf16 v[116:119], a[68:71], v[84:87], v[116:119]\n"
    "ds_read_b64_tr_b16 a[196:197], v239 offset:11328\n"
    "v_cvt_pk_bf16_f32 v99, v102, v103\n"
    "v_mfma_f32_16x16x32_bf16 v[154:157], a[0:3], a[232:235], 0\n"
    "ds_read_b64_tr_b16 a[198:199], v239 offset:11712\n"
    "v_or3_b32 v234, v234, v108, v109\n"
    "v_mfma_f32_16x16x32_bf16 v[154:157], a[4:7], v[64:67], v[154:157]\n"
    "buffer_load_dwordx4 v243, s[48:51], s21 offen offset:2048 lds\n"
    "v_mfma_f32_16x16x32_bf16 v[154:157], a[8:11], v[88:91], v[154:157]\n"
    "s_add_u32 m0, s87, 76800\n"
    "v_or3_b32 v234, v234, v110, v111\n"
    "v_or3_b32 v234, v234, v146, v147\n"
    "v_mfma_f32_16x16x32_bf16 v[154:157], a[12:15], a[236:239], v[154:157]\n"
    "s_add_u32 s21, s31, 12288\n"
    "v_or3_b32 v234, v234, v148, v149\n"
    "v_cmp_ne_u32_e64 s[76:77], 0, v234\n"
    "v_mfma_f32_16x16x32_bf16 v[154:157], a[16:19], v[68:71], v[154:157]\n"
    "v_mul_f32 v104, s82, v104\n"
    "v_mul_f32 v105, s82, v105\n"
    "v_mfma_f32_16x16x32_bf16 v[154:157], a[20:23], v[92:95], v[154:157]\n"
    "s_mov_b64 vcc, s[76:77]\n"
    "v_mul_f32 v106, s82, v106\n"
    "v_mul_f32 v107, s82, v107\n"
    "v_mfma_f32_16x16x32_bf16 v[166:169], a[24:27], a[232:235], 0\n"
    "s_waitcnt vmcnt(12)\n"
    "v_cvt_pk_bf16_f32 v100, v104, v105\n"
    "v_cvt_pk_bf16_f32 v101, v106, v107\n"
    "v_mfma_f32_16x16x32_bf16 v[166:169], a[28:31], v[64:67], v[166:169]\n"
    "s_cbranch_vccnz .Lswin_P1f_%=\n"
    ".Lswin_P1fR_%=:\n"
    "v_mul_f32 v150, s82, v150\n"
    "v_mul_f32 v151, s82, v151\n"
    "v_mfma_f32_16x16x32_bf16 v[166:169], a[32:35], v[88:91], v[166:169]\n"
    "s_waitcnt lgkmcnt(0)\n"
    "buffer_load_dwordx4 v240, s[48:51], s21 offen offset:0 lds\n"
    "v_mfma_f32_16x16x32_bf16 v[166:169], a[36:39], a[236:239], v[166:169]\n"
    "ds_read_b64_tr_b16 a[152:153], v237 offset:57344\n"
    "v_mul_f32 v152, s82, v152\n"
    "v_mfma_f32_16x16x32_bf16 v[166:169], a[40:43], v[68:71], v[166:169]\n"
    "ds_read_b64_tr_b16 a[154:155], v237 offset:57728\n"
    "v_mul_f32 v153, s82, v153\n"
    "v_mfma_f32_16x16x32_bf16 v[166:169], a[44:47], v[92:95], v[166:169]\n"
    "buffer_load_dwordx4 v241, s[48:51], s21 offen offset:1024 lds\n"
    "v_mfma_f32_16x16x32_bf16 v[146:149], a[48:51], a[232:235], 0\n"
    "ds_read_b64_tr_b16 a[176:177], v237 offset:57376\n"
    "v_cvt_pk_bf16_f32 v102, v150, v151\n"
    "v_mfma_f32_16x16x32_bf16 v[146:149], a[52:55], v[64:67], v[146:149]\n"
    "ds_read_b64_tr_b16 a[178:179], v237 offset:57760\n"
    "v_cvt_pk_bf16_f32 v103, v152, v153\n"
    "v_mfma_f32_16x16x32_bf16 v[146:149], a[56:59], v[88:91], v[146:149]\n"
    "buffer_load_dwordx4 v243, s[48:51], s21 offen offset:2048 lds\n"
    "v_mfma_f32_16x16x32_bf16 v[146:149], a[60:63], a[236:239], v[146:149]\n"
    "s_add_u32 m0, s87, 122880\n"
    "v_mul_f32 v112, s82, v112\n"
    "v_mul_f32 v113, s82, v113\n"
    "v_mfma_f32_16x16x32_bf16 v[146:149], a[64:67], v[68:71], v[146:149]\n"
    "s_add_u32 s21, s31, 24576\n"
    "v_mul_f32 v114, s82, v114\n"
    "v_mul_f32 v115, s82, v115\n"
    "v_mfma_f32_16x16x32_bf16 v[146:149], a[68:71], v[92:95], v[146:149]\n"
    "ds_read_b64_tr_b16 a[200:201], v239 offset:57408\n"
    "v_cvt_pk_bf16_f32 v104, v112, v113\n"
    "v_mfma_f32_16x16x32_bf16 v[174:177], a[0:3], v[198:201], 0\n"
    "ds_read_b64_tr_b16 a[202:203], v239 offset:57792\n"
    "v_cvt_pk_bf16_f32 v105, v114, v115\n"
    "v_mfma_f32_16x16x32_bf16 v[174:177], a[4:7], v[128:131], v[174:177]\n"
    "s_xor_b32 s92, s92, 8192\n"
    "v_mul_f32 v116, s82, v116\n"
    "v_mul_f32 v117, s82, v117\n"
    "v_mfma_f32_16x16x32_bf16 v[174:177], a[8:11], v[124:127], v[174:177]\n"
    "v_mul_f32 v118, s82, v118\n"
    "v_mul_f32 v119, s82, v119\n"
    "v_mfma_f32_16x16x32_bf16 v[174:177], a[12:15], v[186:189], v[174:177]\n"
    "v_cvt_pk_bf16_f32 v106, v116, v117\n"
    "v_cvt_pk_bf16_f32 v107, v118, v119\n"
    "v_mfma_f32_16x16x32_bf16 v[174:177], a[16:19], v[190:193], v[174:177]\n"
    "s_waitcnt vmcnt(12)\n"
    "v_mul_f32 v154, s82, v154\n"
    "v_mul_f32 v155, s82, v155\n"
    "v_mfma_f32_16x16x32_bf16 v[174:177], a[20:23], v[194:197], v[174:177]\n"
    "v_mul_f32 v156, s82, v156\n"
    "v_mul_f32 v157, s82, v157\n"
    "v_mfma_f32_16x16x32_bf16 v[170:173], a[24:27], v[198:201], 0\n"
    "ds_read_b64_tr_b16 a[156:157], v237 offset:60416\n"
    "v_cvt_pk_bf16_f32 v108, v154, v155\n"
    "v_mfma_f32_16x16x32_bf16 v[170:173], a[28:31], v[128:131], v[170:173]\n"
    "ds_read_b64_tr_b16 a[158:159], v237 offset:60800\n"
    "v_cvt_pk_bf16_f32 v109, v156, v157\n"
    "v_mfma_f32_16x16x32_bf16 v[170:173], a[32:35], v[124:127], v[170:173]\n"
    "s_waitcnt lgkmcnt(2)\n"
    "buffer_load_dwordx4 v240, s[48:51], s21 offen offset:0 lds\n"
    "v_mfma_f32_16x16x32_bf16 v[170:173], a[36:39], v[186:189], v[170:173]\n"
    "ds_read_b64_tr_b16 a[180:181], v237 offset:60448\n"
    "v_mul_f32 v166, s82, v166\n"
    "v_mfma_f32_16x16x32_bf16 v[170:173], a[40:43], v[190:193], v[170:173]\n"
    "ds_read_b64_tr_b16 a[182:183], v237 offset:60832\n"
    "v_mul_f32 v167, s82, v167\n"
    "v_mfma_f32_16x16x32_bf16 v[170:173], a[44:47], v[194:197], v[170:173]\n"
    "buffer_load_dwordx4 v241, s[48:51], s21 offen offset:1024 lds\n"
    "v_mfma_f32_16x16x32_bf16 v[154:157], a[48:51], v[198:201], 0\n"
    "ds_read_b64_tr_b16 a[204:205], v239 offset:60480\n"
    "v_mul_f32 v168, s82, v168\n"
    "v_mfma_f32_16x16x32_bf16 v[154:157], a[52:55], v[128:131], v[154:157]\n"
    "ds_read_b64_tr_b16 a[206:207], v239 offset:60864\n"
    "v_mul_f32 v169, s82, v169\n"
    "v_mfma_f32_16x16x32_bf16 v[154:157], a[56:59], v[124:127], v[154:157]\n"
    "buffer_load_dwordx4 v243, s[48:51], s21 offen offset:2048 lds\n"
    "v_mfma_f32_16x16x32_bf16 v[154:157], a[60:63], v[186:189], v[154:157]\n"
    "s_add_u32 m0, s87, 125952\n"
    "v_cvt_pk_bf16_f32 v110, v166, v167\n"
    "v_cvt_pk_bf16_f32 v111, v168, v169\n"
    "v_mfma_f32_16x16x32_bf16 v[154:157], a[64:67], v[190:193], v[154:157]\n"
    "s_add_u32 s21, s31, 36864\n"
    "v_mul_f32 v146, s82, v146\n"
    "v_mul_f32 v147, s82, v147\n"
    "v_mfma_f32_16x16x32_bf16 v[154:157], a[68:71], v[194:197], v[154:157]\n"
    "v_mul_f32 v148, s82, v148\n"
    "v_mul_f32 v149, s82, v149\n"
    "v_mfma_f32_16x16x32_bf16 v[202:205], v[158:161], v[96:99], v[0:3]\n"
    "v_cvt_pk_bf16_f32 v112, v146, v147\n"
    "v_cvt_pk_bf16_f32 v113, v148, v149\n"
    "v_mfma_f32_16x16x32_bf16 v[202:205], v[134:137], v[100:103], v[202:205]\n"
    "v_mul_f32 v174, s82, v174\n"
    "v_mul_f32 v175, s82, v175\n"
    "v_mfma_f32_16x16x32_bf16 v[150:153], v[162:165], v[96:99], v[4:7]\n"
    "s_waitcnt vmcnt(12)\n"
    "v_mul_f32 v176, s82, v176\n"
    "v_mul_f32 v177, s82, v177\n"
    "v_mfma_f32_16x16x32_bf16 v[150:153], v[136:139], v[98:101], v[150:153]\n"
    "s_waitcnt lgkmcnt(0)\n"
    "buffer_load_dwordx4 v240, s[48:51], s21 offen offset:0 lds\n"
    "v_mfma_f32_16x16x32_bf16 v[226:229], v[178:181], v[96:99], v[8:11]\n"
    "ds_read_b64_tr_b16 a[160:161], v237 offset:32768\n"
    "v_cvt_pk_bf16_f32 v114, v174, v175\n"
    "v_mfma_f32_16x16x32_bf16 v[226:229], v[140:143], v[100:103], v[226:229]\n"
    "ds_read_b64_tr_b16 a[162:163], v237 offset:33152\n"
    "v_cvt_pk_bf16_f32 v115, v176, v177\n"
    "v_mfma_f32_16x16x32_bf16 v[146:149], v[182:185], v[96:99], v[12:15]\n"
    "buffer_load_dwordx4 v241, s[48:51], s21 offen offset:1024 lds\n"
    "v_mfma_f32_16x16x32_bf16 v[146:149], v[142:145], v[98:101], v[146:149]\n"
    "ds_read_b64_tr_b16 a[184:185], v237 offset:32800\n"
    "v_mul_f32 v170, s82, v170\n"
    "v_mfma_f32_16x16x32_bf16 v[218:221], v[158:161], v[102:105], v[16:19]\n"
    "ds_read_b64_tr_b16 a[186:187], v237 offset:33184\n"
    "v_mul_f32 v171, s82, v171\n"
    "v_mfma_f32_16x16x32_bf16 v[218:221], v[134:137], v[106:109], v[218:221]\n"
    "buffer_load_dwordx4 v243, s[48:51], s21 offen offset:2048 lds\n"
    "v_mfma_f32_16x16x32_bf16 v[174:177], v[162:165], v[102:105], v[20:23]\n"
    "s_add_u32 m0, s87, 98304\n"
    "v_mul_f32 v172, s82, v172\n"
    "v_mul_f32 v173, s82, v173\n"
    "v_mfma_f32_16x16x32_bf16 v[174:177], v[136:139], v[104:107], v[174:177]\n"
    "s_add_u32 s21, s31, 49152\n"
    "v_cvt_pk_bf16_f32 v116, v170, v171\n"
    "v_cvt_pk_bf16_f32 v117, v172, v173\n"
    "v_mfma_f32_16x16x32_bf16 v[222:225], v[178:181], v[102:105], v[24:27]\n"
    "ds_read_b64_tr_b16 a[208:209], v239 offset:32832\n"
    "v_mul_f32 v154, s82, v154\n"
    "v_mfma_f32_16x16x32_bf16 v[222:225], v[140:143], v[106:109], v[222:225]\n"
    "ds_read_b64_tr_b16 a[210:211], v239 offset:33216\n"
    "v_mul_f32 v155, s82, v155\n"
    "v_mfma_f32_16x16x32_bf16 v[214:217], v[182:185], v[102:105], v[28:31]\n"
    "v_exp_f32 v202, v202\n"
    "v_mfma_f32_16x16x32_bf16 v[214:217], v[142:145], v[104:107], v[214:217]\n"
    "v_exp_f32 v203, v203\n"
    "v_mfma_f32_16x16x32_bf16 v[100:103], v[158:161], v[108:111], v[32:35]\n"
    "v_mul_f32 v156, s82, v156\n"
    "v_mul_f32 v157, s82, v157\n"
    "v_mfma_f32_16x16x32_bf16 v[100:103], v[134:137], v[112:115], v[100:103]\n"
    "v_cvt_pk_bf16_f32 v118, v154, v155\n"
    "v_cvt_pk_bf16_f32 v119, v156, v157\n"
    "v_mfma_f32_16x16x32_bf16 v[206:209], v[162:165], v[108:111], v[36:39]\n"
    "s_waitcnt vmcnt(12)\n"
    "v_cvt_pk_bf16_f32 v202, v202, v203\n"
    "v_mfma_f32_16x16x32_bf16 v[206:209], v[136:139], v[110:113], v[206:209]\n"
    "v_exp_f32 v204, v204\n"
    "v_mfma_f32_16x16x32_bf16 v[210:213], v[178:181], v[108:111], v[40:43]\n"
    "ds_read_b64_tr_b16 a[164:165], v237 offset:35840\n"
    "v_mfma_f32_16x16x32_bf16 v[210:213], v[140:143], v[112:115], v[210:213]\n"
    "s_waitcnt lgkmcnt(1)\n"
    "buffer_load_dwordx4 v240, s[48:51], s21 offen offset:0 lds\n"
    "v_mfma_f32_16x16x32_bf16 v[230:233], v[182:185], v[108:111], v[44:47]\n"
    "ds_read_b64_tr_b16 a[166:167], v237 offset:36224\n"
    "v_mfma_f32_16x16x32_bf16 v[230:233], v[142:145], v[110:113], v[230:233]\n"
    "ds_read_b64_tr_b16 a[188:189], v237 offset:35872\n"
    "v_mfma_f32_16x16x32_bf16 v[108:111], v[158:161], v[114:117], v[48:51]\n"
    "buffer_load_dwordx4 v241, s[48:51], s21 offen offset:1024 lds\n"
    "v_mfma_f32_16x16x32_bf16 v[108:111], v[134:137], v[118:121], v[108:111]\n"
    "ds_read_b64_tr_b16 a[190:191], v237 offset:36256\n"
    "v_mfma_f32_16x16x32_bf16 v[132:135], v[162:165], v[114:117], v[52:55]\n"
    "ds_read_b64_tr_b16 a[212:213], v239 offset:35904\n"
    "v_mfma_f32_16x16x32_bf16 v[132:135], v[136:139], v[116:119], v[132:135]\n"
    "buffer_load_dwordx4 v243, s[48:51], s21 offen offset:2048 lds\n"
    "v_mfma_f32_16x16x32_bf16 v[104:107], v[178:181], v[114:117], v[56:59]\n"
    "s_add_u32 m0, s87, 101376\n"
    "v_exp_f32 v205, v205\n"
    "v_mfma_f32_16x16x32_bf16 v[104:107], v[140:143], v[118:121], v[104:107]\n"
    "s_add_u32 s21, s31, 61440\n"
    "v_cvt_pk_bf16_f32 v203, v204, v205\n"
    "v_mfma_f32_16x16x32_bf16 v[96:99], v[182:185], v[114:117], v[60:63]\n"
    "ds_read_b64_tr_b16 a[214:215], v239 offset:36288\n"
    "v_mfma_f32_16x16x32_bf16 v[96:99], v[142:145], v[116:119], v[96:99]\n"
    "v_exp_f32 v150, v150\n"
    "v_mfma_f32_16x16x32_bf16 v[138:141], a[216:219], a[144:147], 0\n"
    "s_cbranch_vccnz .Lswin_P2f_%=\n"
    ".Lswin_P2fR_%=:\n"
    "v_exp_f32 v151, v151\n"
    "v_mfma_f32_16x16x32_bf16 v[138:141], a[240:243], a[148:151], v[138:141]\n"
    "s_add_u32 s84, s84, 1\n"
    "v_cvt_pk_bf16_f32 v204, v150, v151\n"
    "v_mfma_f32_16x16x32_bf16 v[138:141], v[72:75], a[152:155], v[138:141]\n"
    "v_exp_f32 v152, v152\n"
    "v_mfma_f32_16x16x32_bf16 v[138:141], a[220:223], a[156:159], v[138:141]\n"
    "s_waitcnt lgkmcnt(1)\n"
    "v_exp_f32 v153, v153\n"
    "v_mfma_f32_16x16x32_bf16 v[138:141], a[244:247], a[160:163], v[138:141]\n"
    "s_waitcnt vmcnt(12)\n"
    "v_cvt_pk_bf16_f32 v205, v152, v153\n"
    "s_waitcnt vmcnt(9)\n"
    "v_mfma_f32_16x16x32_bf16 v[138:141], v[76:79], a[164:167], v[138:141]\n"
    "s_waitcnt vmcnt(6)\n"
    "v_exp_f32 v226, v226\n"
    "v_mfma_f32_16x16x32_bf16 v[116:119], a[216:219], a[168:171], 0\n"
    "s_waitcnt lgkmcnt(0)\n"
    "buffer_load_dwordx4 v240, s[48:51], s21 offen offset:0 lds\n"
    "v_mfma_f32_16x16x32_bf16 v[116:119], a[240:243], a[172:175], v[116:119]\n"
    "v_exp_f32 v227, v227\n"
    "v_mfma_f32_16x16x32_bf16 v[116:119], v[72:75], a[176:179], v[116:119]\n"
    "v_cvt_pk_bf16_f32 v226, v226, v227\n"
    "v_cvt_pk_bf16_f32 v150, v138, v139\n"
    "v_mfma_f32_16x16x32_bf16 v[116:119], a[220:223], a[180:183], v[116:119]\n"
    "buffer_load_dwordx4 v241, s[48:51], s21 offen offset:1024 lds\n"
    "v_mfma_f32_16x16x32_bf16 v[116:119], a[244:247], a[184:187], v[116:119]\n"
    "s_waitcnt vmcnt(5)\n"
    "v_exp_f32 v228, v228\n"
    "v_mfma_f32_16x16x32_bf16 v[116:119], v[76:79], a[188:191], v[116:119]\n"
    "v_exp_f32 v229, v229\n"
    "v_mfma_f32_16x16x32_bf16 v[112:115], a[216:219], a[192:195], 0\n"
    "buffer_load_dwordx4 v243, s[48:51], s21 offen offset:2048 lds\n"
    "v_mfma_f32_16x16x32_bf16 v[112:115], a[240:243], a[196:199], v[112:115]\n"
    "s_add_u32 m0, s87, 24576\n"
    "v_cvt_pk_bf16_f32 v227, v228, v229\n"
    "v_cvt_pk_bf16_f32 v151, v140, v141\n"
    "v_mfma_f32_16x16x32_bf16 v[112:115], v[72:75], a[200:203], v[112:115]\n"
    "v_exp_f32 v146, v146\n"
    "v_mfma_f32_16x16x32_bf16 v[112:115], a[220:223], a[204:207], v[112:115]\n"
    "buffer_load_dwordx4 v251, s[36:39], s20 offen offset:0 lds\n"
    "v_mfma_f32_16x16x32_bf16 v[112:115], a[244:247], a[208:211], v[112:115]\n"
    "v_exp_f32 v147, v147\n"
    "v_mfma_f32_16x16x32_bf16 v[112:115], v[76:79], a[212:215], v[112:115]\n"
    "v_cvt_pk_bf16_f32 v228, v146, v147\n"
    "v_cvt_pk_bf16_f32 v146, v116, v117\n"
    "v_mfma_f32_16x16x32_bf16 v[138:141], a[224:227], a[144:147], 0\n"
    "buffer_load_dwordx4 v249, s[36:39], s20 offen offset:1024 lds\n"
    "v_mfma_f32_16x16x32_bf16 v[138:141], a[248:251], a[148:151], v[138:141]\n"
    "v_exp_f32 v148, v148\n"
    "v_mfma_f32_16x16x32_bf16 v[138:141], v[80:83], a[152:155], v[138:141]\n"
    "v_exp_f32 v149, v149\n"
    "v_mfma_f32_16x16x32_bf16 v[138:141], a[228:231], a[156:159], v[138:141]\n"
    "buffer_load_dwordx4 v247, s[36:39], s20 offen offset:2048 lds\n"
    "v_mfma_f32_16x16x32_bf16 v[138:141], a[252:255], a[160:163], v[138:141]\n"
    "s_add_u32 s20, s62, s86\n"
    "v_cvt_pk_bf16_f32 v229, v148, v149\n"
    "v_cvt_pk_bf16_f32 v147, v118, v119\n"
    "v_mfma_f32_16x16x32_bf16 v[138:141], v[84:87], a[164:167], v[138:141]\n"
    "s_add_u32 s20, s20, 147456\n"
    "v_exp_f32 v218, v218\n"
    "v_mfma_f32_16x16x32_bf16 v[116:119], a[224:227], a[168:171], 0\n"
    "s_add_u32 m0, s87, 27648\n"
    "v_exp_f32 v219, v219\n"
    "v_mfma_f32_16x16x32_bf16 v[116:119], a[248:251], a[172:175], v[116:119]\n"
    "buffer_load_dwordx4 v251, s[36:39], s20 offen offset:0 lds\n"
    "v_mfma_f32_16x16x32_bf16 v[116:119], v[80:83], a[176:179], v[116:119]\n"
    "ds_read_b64_tr_b16 a[216:217], v242 offset:8192\n"
    "v_cvt_pk_bf16_f32 v218, v218, v219\n"
    "v_mfma_f32_16x16x32_bf16 v[116:119], a[228:231], a[180:183], v[116:119]\n"
    "ds_read_b64_tr_b16 a[218:219], v242 offset:9728\n"
    "v_cvt_pk_bf16_f32 v154, v112, v113\n"
    "v_mfma_f32_16x16x32_bf16 v[116:119], a[252:255], a[184:187], v[116:119]\n"
    "buffer_load_dwordx4 v249, s[36:39], s20 offen offset:1024 lds\n"
    "v_mfma_f32_16x16x32_bf16 v[116:119], v[84:87], a[188:191], v[116:119]\n"
    "ds_read_b64_tr_b16 a[240:241], v242 offset:8200\n"
    "v_cvt_pk_bf16_f32 v155, v114, v115\n"
    "v_mfma_f32_16x16x32_bf16 v[112:115], a[224:227], a[192:195], 0\n"
    "ds_read_b64_tr_b16 a[242:243], v242 offset:9736\n"
    "v_cvt_pk_bf16_f32 v152, v138, v139\n"
    "v_mfma_f32_16x16x32_bf16 v[112:115], a[248:251], a[196:199], v[112:115]\n"
    "buffer_load_dwordx4 v247, s[36:39], s20 offen offset:2048 lds\n"
    "v_mfma_f32_16x16x32_bf16 v[112:115], v[80:83], a[200:203], v[112:115]\n"
    "s_add_u32 m0, s88, 155648\n"
    "v_exp_f32 v220, v220\n"
    "v_mfma_f32_16x16x32_bf16 v[112:115], a[228:231], a[204:207], v[112:115]\n"
    "ds_read_b64_tr_b16 v[72:73], v244 offset:8192\n"
    "v_cvt_pk_bf16_f32 v153, v140, v141\n"
    "v_mfma_f32_16x16x32_bf16 v[112:115], a[252:255], a[208:211], v[112:115]\n"
    "buffer_load_dwordx4 v250, s[44:47], s63 offen offset:0 lds\n"
    "v_mfma_f32_16x16x32_bf16 v[112:115], v[84:87], a[212:215], v[112:115]\n"
    "ds_read_b64_tr_b16 v[74:75], v244 offset:9728\n"
    "v_cvt_pk_bf16_f32 v148, v116, v117\n"
    "v_mfma_f32_16x16x32_bf16 v[138:141], a[232:235], a[144:147], 0\n"
    "ds_read_b64_tr_b16 a[220:221], v242 offset:11264\n"
    "v_cvt_pk_bf16_f32 v149, v118, v119\n"
    "v_mfma_f32_16x16x32_bf16 v[138:141], v[64:67], a[148:151], v[138:141]\n"
    "buffer_load_dwordx4 v250, s[44:47], s63 offen offset:1024 lds\n"
    "v_mfma_f32_16x16x32_bf16 v[138:141], v[88:91], a[152:155], v[138:141]\n"
    "ds_read_b64_tr_b16 a[222:223], v242 offset:12800\n"
    "v_cvt_pk_bf16_f32 v156, v112, v113\n"
    "v_mfma_f32_16x16x32_bf16 v[138:141], a[236:239], a[156:159], v[138:141]\n"
    "ds_read_b64_tr_b16 a[244:245], v242 offset:11272\n"
    "v_cvt_pk_bf16_f32 v157, v114, v115\n"
    "v_mfma_f32_16x16x32_bf16 v[138:141], v[68:71], a[160:163], v[138:141]\n"
    "ds_read_b64_tr_b16 a[246:247], v242 offset:12808\n"
    "s_waitcnt vmcnt(11)\n"
    "v_mfma_f32_16x16x32_bf16 v[138:141], v[92:95], a[164:167], v[138:141]\n"
    "ds_read_b64_tr_b16 v[76:77], v244 offset:11264\n"
    "s_add_u32 s20, s64, s86\n"
    "v_mfma_f32_16x16x32_bf16 v[116:119], a[232:235], a[168:171], 0\n"
    "ds_read_b64_tr_b16 v[78:79], v244 offset:12800\n"
    "s_add_u32 m0, s87, 49152\n"
    "v_mfma_f32_16x16x32_bf16 v[116:119], v[64:67], a[172:175], v[116:119]\n"
    "v_exp_f32 v221, v221\n"
    "v_mfma_f32_16x16x32_bf16 v[116:119], v[88:91], a[176:179], v[116:119]\n"
    "ds_read_b64_tr_b16 a[224:225], v242 offset:57344\n"
    "v_cvt_pk_bf16_f32 v219, v220, v221\n"
    "v_mfma_f32_16x16x32_bf16 v[116:119], a[236:239], a[180:183], v[116:119]\n"
    "ds_read_b64_tr_b16 a[226:227], v242 offset:58880\n"
    "v_cvt_pk_bf16_f32 v170, v138, v139\n"
    "v_mfma_f32_16x16x32_bf16 v[116:119], v[68:71], a[184:187], v[116:119]\n"
    "ds_read_b64_tr_b16 a[248:249], v242 offset:57352\n"
    "v_cvt_pk_bf16_f32 v171, v140, v141\n"
    "v_mfma_f32_16x16x32_bf16 v[116:119], v[92:95], a[188:191], v[116:119]\n"
    "ds_read_b64_tr_b16 a[250:251], v242 offset:58888\n"
    "s_waitcnt vmcnt(8)\n"
    "v_mfma_f32_16x16x32_bf16 v[112:115], a[232:235], a[192:195], 0\n"
    "ds_read_b64_tr_b16 v[80:81], v244 offset:57344\n"
    "v_mfma_f32_16x16x32_bf16 v[112:115], v[64:67], a[196:199], v[112:115]\n"
    "ds_read_b64_tr_b16 v[82:83], v244 offset:58880\n"
    "v_cvt_pk_bf16_f32 v166, v116, v117\n"
    "v_mfma_f32_16x16x32_bf16 v[112:115], v[88:91], a[200:203], v[112:115]\n"
    "v_exp_f32 v174, v174\n"
    "v_mfma_f32_16x16x32_bf16 v[112:115], a[236:239], a[204:207], v[112:115]\n"
    "v_exp_f32 v175, v175\n"
    "v_mfma_f32_16x16x32_bf16 v[112:115], v[68:71], a[208:211], v[112:115]\n"
    "v_cvt_pk_bf16_f32 v220, v174, v175\n"
    "v_cvt_pk_bf16_f32 v167, v118, v119\n"
    "v_mfma_f32_16x16x32_bf16 v[112:115], v[92:95], a[212:215], v[112:115]\n"
    "ds_read_b64_tr_b16 a[228:229], v242 offset:60416\n"
    "v_mfma_f32_16x16x32_bf16 v[116:119], v[198:201], a[144:147], 0\n"
    "ds_read_b64_tr_b16 a[230:231], v242 offset:61952\n"
    "v_mfma_f32_16x16x32_bf16 v[116:119], v[128:131], a[148:151], v[116:119]\n"
    "ds_read_b64_tr_b16 a[252:253], v242 offset:60424\n"
    "v_cvt_pk_bf16_f32 v174, v112, v113\n"
    "v_mfma_f32_16x16x32_bf16 v[116:119], v[124:127], a[152:155], v[116:119]\n"
    "ds_read_b64_tr_b16 a[254:255], v242 offset:61960\n"
    "v_cvt_pk_bf16_f32 v175, v114, v115\n"
    "v_mfma_f32_16x16x32_bf16 v[116:119], v[186:189], a[156:159], v[116:119]\n"
    "ds_read_b64_tr_b16 v[84:85], v244 offset:60416\n"
    "v_mfma_f32_16x16x32_bf16 v[116:119], v[190:193], a[160:163], v[116:119]\n"
    "ds_read_b64_tr_b16 v[86:87], v244 offset:61952\n"
    "v_mfma_f32_16x16x32_bf16 v[116:119], v[194:197], a[164:167], v[116:119]\n"
    "v_exp_f32 v176, v176\n"
    "v_mfma_f32_16x16x32_bf16 v[138:141], v[198:201], a[168:171], 0\n"
    "v_exp_f32 v177, v177\n"
    "v_mfma_f32_16x16x32_bf16 v[138:141], v[128:131], a[172:175], v[138:141]\n"
    "v_cvt_pk_bf16_f32 v221, v176, v177\n"
    "v_cvt_pk_bf16_f32 v172, v116, v117\n"
    "v_mfma_f32_16x16x32_bf16 v[138:141], v[124:127], a[176:179], v[138:141]\n"
    "ds_read_b64_tr_b16 a[232:233], v242 offset:32768\n"
    "v_cvt_pk_bf16_f32 v173, v118, v119\n"
    "v_mfma_f32_16x16x32_bf16 v[138:141], v[186:189], a[180:183], v[138:141]\n"
    "ds_read_b64_tr_b16 a[234:235], v242 offset:34304\n"
    "v_mfma_f32_16x16x32_bf16 v[138:141], v[190:193], a[184:187], v[138:141]\n"
    "ds_read_b64_tr_b16 v[64:65], v242 offset:32776\n"
    "v_mfma_f32_16x16x32_bf16 v[138:141], v[194:197], a[188:191], v[138:141]\n"
    "ds_read_b64_tr_b16 v[66:67], v242 offset:34312\n"
    "v_mfma_f32_16x16x32_bf16 v[112:115], v[198:201], a[192:195], 0\n"
    "ds_read_b64_tr_b16 v[88:89], v244 offset:32768\n"
    "v_mfma_f32_16x16x32_bf16 v[112:115], v[128:131], a[196:199], v[112:115]\n"
    "ds_read_b64_tr_b16 v[90:91], v244 offset:34304\n"
    "v_cvt_pk_bf16_f32 v168, v138, v139\n"
    "v_mfma_f32_16x16x32_bf16 v[112:115], v[124:127], a[200:203], v[112:115]\n"
    "v_exp_f32 v222, v222\n"
    "v_mfma_f32_16x16x32_bf16 v[112:115], v[186:189], a[204:207], v[112:115]\n"
    "v_exp_f32 v223, v223\n"
    "v_mfma_f32_16x16x32_bf16 v[112:115], v[190:193], a[208:211], v[112:115]\n"
    "v_cvt_pk_bf16_f32 v222, v222, v223\n"
    "v_cvt_pk_bf16_f32 v169, v140, v141\n"
    "v_mfma_f32_16x16x32_bf16 v[112:115], v[194:197], a[212:215], v[112:115]\n"
    "ds_read_b64_tr_b16 a[236:237], v242 offset:35840\n"
    "v_mfma_f32_16x16x32_bf16 v[234:237], v[120:123], v[202:205], 0\n"
    "ds_read_b64_tr_b16 a[238:239], v242 offset:37376\n"
    "v_mfma_f32_16x16x32_bf16 v[234:237], v[120:123], v[226:229], v[234:237]\n"
    "ds_read_b64_tr_b16 v[68:69], v242 offset:35848\n"
    "v_cvt_pk_bf16_f32 v176, v112, v113\n"
    "v_mfma_f32_16x16x32_bf16 v[162:165], v[150:153], v[202:205], 0\n"
    "ds_read_b64_tr_b16 v[70:71], v242 offset:37384\n"
    "v_cvt_pk_bf16_f32 v177, v114, v115\n"
    "v_mfma_f32_16x16x32_bf16 v[162:165], v[170:173], v[226:229], v[162:165]\n"
    "ds_read_b64_tr_b16 v[92:93], v244 offset:35840\n"
    "v_mfma_f32_16x16x32_bf16 v[198:201], v[146:149], v[202:205], 0\n"
    "ds_read_b64_tr_b16 v[94:95], v244 offset:37376\n"
    "v_mfma_f32_16x16x32_bf16 v[198:201], v[166:169], v[226:229], v[198:201]\n"
    "v_exp_f32 v224, v224\n"
    "v_mfma_f32_16x16x32_bf16 v[158:161], v[154:157], v[202:205], 0\n"
    "v_exp_f32 v225, v225\n"
    "v_mfma_f32_16x16x32_bf16 v[158:161], v[174:177], v[226:229], v[158:161]\n"
    "v_cvt_pk_bf16_f32 v223, v224, v225\n"
    "v_mfma_f32_16x16x32_bf16 v[226:229], v[120:123], v[218:221], 0\n"
    "v_exp_f32 v214, v214\n"
    "v_exp_f32 v215, v215\n"
    "s_nop 0\n"
    "v_cvt_pk_bf16_f32 v224, v214, v215\n"
    "v_exp_f32 v216, v216\n"
    "v_exp_f32 v217, v217\n"
    "s_nop 0\n"
    "v_cvt_pk_bf16_f32 v225, v216, v217\n"
    "s_nop 1\n"
    "v_mfma_f32_16x16x32_bf16 v[226:229], v[120:123], v[222:225], v[226:229]\n"
    "s_waitcnt vmcnt(2) lgkmcnt(0)\n"
    "v_exp_f32 v100, v100\n"
    "v_mfma_f32_16x16x32_bf16 v[194:197], v[150:153], v[218:221], 0\n"
    "s_barrier\n"
    "v_exp_f32 v101, v101\n"
    "v_mfma_f32_16x16x32_bf16 v[194:197], v[170:173], v[222:225], v[194:197]\n"
    "buffer_load_dwordx4 v251, s[36:39], s20 offen offset:0 lds\n"
    "v_mfma_f32_16x16x32_bf16 v[202:205], v[146:149], v[218:221], 0\n"
    "v_cvt_pk_bf16_f32 v100, v100, v101\n"
    "v_mfma_f32_16x16x32_bf16 v[202:205], v[166:169], v[222:225], v[202:205]\n"
    "v_exp_f32 v102, v102\n"
    "v_mfma_f32_16x16x32_bf16 v[186:189], v[154:157], v[218:221], 0\n"
    "buffer_load_dwordx4 v249, s[36:39], s20 offen offset:1024 lds\n"
    "v_mfma_f32_16x16x32_bf16 v[186:189], v[174:177], v[222:225], v[186:189]\n"
    "v_exp_f32 v103, v103\n"
    "s_nop 0\n"
    "v_cvt_pk_bf16_f32 v101, v102, v103\n"
    "v_exp_f32 v206, v206\n"
    "v_exp_f32 v207, v207\n"
    "s_nop 0\n"
    "v_cvt_pk_bf16_f32 v102, v206, v207\n"
    "v_exp_f32 v208, v208\n"
    "v_exp_f32 v209, v209\n"
    "s_nop 0\n"
    "v_cvt_pk_bf16_f32 v103, v208, v209\n"
    "s_nop 1\n"
    "v_mfma_f32_16x16x32_bf16 v[236:239], v[120:123], v[100:103], 0\n"
    "v_exp_f32 v210, v210\n"
    "v_exp_f32 v211, v211\n"
    "s_nop 0\n"
    "v_cvt_pk_bf16_f32 v210, v210, v211\n"
    "v_exp_f32 v212, v212\n"
    "v_exp_f32 v213, v213\n"
    "s_nop 0\n"
    "v_cvt_pk_bf16_f32 v211, v212, v213\n"
    "v_exp_f32 v230, v230\n"
    "v_exp_f32 v231, v231\n"
    "s_nop 0\n"
    "v_cvt_pk_bf16_f32 v212, v230, v231\n"
    "v_exp_f32 v232, v232\n"
    "v_exp_f32 v233, v233\n"
    "s_nop 0\n"
    "v_cvt_pk_bf16_f32 v213, v232, v233\n"
    "s_nop 1\n"
    "v_mfma_f32_16x16x32_bf16 v[236:239], v[120:123], v[210:213], v[236:239]\n"
    "buffer_load_dwordx4 v247, s[36:39], s20 offen offset:2048 lds\n"
    "v_mfma_f32_16x16x32_bf16 v[190:193], v[150:153], v[100:103], 0\n"
    "s_add_u32 s20, s64, s86\n"
    "v_exp_f32 v108, v108\n"
    "v_mfma_f32_16x16x32_bf16 v[190:193], v[170:173], v[210:213], v[190:193]\n"
    "s_add_u32 s20, s20, 147456\n"
    "v_exp_f32 v109, v109\n"
    "v_mfma_f32_16x16x32_bf16 v[206:209], v[146:149], v[100:103], 0\n"
    "s_add_u32 m0, s87, 52224\n"
    "v_cvt_pk_bf16_f32 v108, v108, v109\n"
    "v_mfma_f32_16x16x32_bf16 v[206:209], v[166:169], v[210:213], v[206:209]\n"
    "buffer_load_dwordx4 v251, s[36:39], s20 offen offset:0 lds\n"
    "v_mfma_f32_16x16x32_bf16 v[182:185], v[154:157], v[100:103], 0\n"
    "ds_read_b128 v[138:141], v254 offset:0\n"
    "v_mfma_f32_16x16x32_bf16 v[182:185], v[174:177], v[210:213], v[182:185]\n"
    "ds_read_b128 v[100:103], v248 offset:0\n"
    "v_exp_f32 v110, v110\n"
    "v_exp_f32 v111, v111\n"
    "s_nop 0\n"
    "v_cvt_pk_bf16_f32 v109, v110, v111\n"
    "v_exp_f32 v132, v132\n"
    "v_exp_f32 v133, v133\n"
    "s_nop 0\n"
    "v_cvt_pk_bf16_f32 v110, v132, v133\n"
    "v_exp_f32 v134, v134\n"
    "v_exp_f32 v135, v135\n"
    "s_nop 0\n"
    "v_cvt_pk_bf16_f32 v111, v134, v135\n"
    "s_nop 1\n"
    "v_mfma_f32_16x16x32_bf16 v[132:135], v[120:123], v[108:111], 0\n"
    "buffer_load_dwordx4 v249, s[36:39], s20 offen offset:1024 lds\n"
    "v_exp_f32 v104, v104\n"
    "v_exp_f32 v105, v105\n"
    "s_nop 0\n"
    "v_cvt_pk_bf16_f32 v104, v104, v105\n"
    "v_exp_f32 v106, v106\n"
    "v_exp_f32 v107, v107\n"
    "s_nop 0\n"
    "v_cvt_pk_bf16_f32 v105, v106, v107\n"
    "v_exp_f32 v96, v96\n"
    "v_exp_f32 v97, v97\n"
    "s_nop 0\n"
    "v_cvt_pk_bf16_f32 v106, v96, v97\n"
    "v_exp_f32 v98, v98\n"
    "v_exp_f32 v99, v99\n"
    "s_nop 0\n"
    "v_cvt_pk_bf16_f32 v107, v98, v99\n"
    "s_nop 1\n"
    "v_mfma_f32_16x16x32_bf16 v[132:135], v[120:123], v[104:107], v[132:135]\n"
    "ds_read_b128 v[124:127], v254 offset:128\n"
    "v_mfma_f32_16x16x32_bf16 v[178:181], v[150:153], v[108:111], 0\n"
    "ds_read_b128 v[112:115], v248 offset:128\n"
    "v_mfma_f32_16x16x32_bf16 v[178:181], v[170:173], v[104:107], v[178:181]\n"
    "buffer_load_dwordx4 v247, s[36:39], s20 offen offset:2048 lds\n"
    "v_mfma_f32_16x16x32_bf16 v[150:153], v[146:149], v[108:111], 0\n"
    "ds_read_b128 v[116:119], v254 offset:256\n"
    "v_mfma_f32_16x16x32_bf16 v[150:153], v[166:169], v[104:107], v[150:153]\n"
    "ds_read_b128 v[128:131], v248 offset:256\n"
    "v_mfma_f32_16x16x32_bf16 v[144:147], v[154:157], v[108:111], 0\n"
    "buffer_store_dword v250, v250, s[40:43], s58 offen\n"
    "v_mfma_f32_16x16x32_bf16 v[144:147], v[174:177], v[104:107], v[144:147]\n"
    "v_rcp_f32 v235, v234\n"
    "s_nop 0\n"
    "v_max3_f32 v255, v255, v234, v235\n"
    "v_mul_f32 v162, v235, v162\n"
    "v_mul_f32 v163, v235, v163\n"
    "v_cvt_pk_bf16_f32 v162, v162, v163\n"
    "v_mul_f32 v164, v235, v164\n"
    "v_mul_f32 v165, v235, v165\n"
    "v_cvt_pk_bf16_f32 v163, v164, v165\n"
    "v_mul_f32 v198, v235, v198\n"
    "v_mul_f32 v199, v235, v199\n"
    "v_cvt_pk_bf16_f32 v164, v198, v199\n"
    "v_mul_f32 v200, v235, v200\n"
    "v_mul_f32 v201, v235, v201\n"
    "v_cvt_pk_bf16_f32 v165, v200, v201\n"
    "ds_write_b128 v246, v[162:165] offset:0\n"
    "v_mul_f32 v158, v235, v158\n"
    "v_mul_f32 v159, v235, v159\n"
    "v_cvt_pk_bf16_f32 v158, v158, v159\n"
    "v_mul_f32 v160, v235, v160\n"
    "v_mul_f32 v161, v235, v161\n"
    "v_cvt_pk_bf16_f32 v159, v160, v161\n"
    "ds_write_b64 v252, v[158:159] offset:0\n"
    "v_rcp_f32 v227, v226\n"
    "s_nop 0\n"
    "v_max3_f32 v255, v255, v226, v227\n"
    "v_mul_f32 v194, v227, v194\n"
    "v_mul_f32 v195, v227, v195\n"
    "v_cvt_pk_bf16_f32 v194, v194, v195\n"
    "v_mul_f32 v196, v227, v196\n"
    "v_mul_f32 v197, v227, v197\n"
    "v_cvt_pk_bf16_f32 v195, v196, v197\n"
    "v_mul_f32 v202, v227, v202\n"
    "v_mul_f32 v203, v227, v203\n"
    "v_cvt_pk_bf16_f32 v196, v202, v203\n"
    "v_mul_f32 v204, v227, v204\n"
    "v_mul_f32 v205, v227, v205\n"
    "v_cvt_pk_bf16_f32 v197, v204, v205\n"
    "ds_write_b128 v246, v[194:197] offset:6144\n"
    "v_mul_f32 v186, v227, v186\n"
    "v_mul_f32 v187, v227, v187\n"
    "v_cvt_pk_bf16_f32 v186, v186, v187\n"
    "v_mul_f32 v188, v227, v188\n"
    "v_mul_f32 v189, v227, v189\n"
    "v_cvt_pk_bf16_f32 v187, v188, v189\n"
    "ds_write_b64 v252, v[186:187] offset:6144\n"
    "buffer_store_dword v250, v250, s[40:43], s58 offen\n"
    "buffer_store_dword v250, v250, s[40:43], s58 offen\n"
    "buffer_store_dword v250, v250, s[40:43], s58 offen\n"
    "buffer_store_dword v250, v250, s[40:43], s58 offen\n"
    "buffer_store_dword v250, v250, s[40:43], s58 offen\n"
    "s_mov_b64 s[58:59], s[60:61]\n"
    "s_mov_b64 s[60:61], s[62:63]\n"
    "s_mov_b64 s[62:63], s[64:65]\n"
    "s_mul_i32 s64, s28, 1179648\n"
    "s_add_u32 s64, s64, s30\n"
    "s_add_u32 s64, s64, 591360\n"
    "s_mul_i32 s65, s28, 48\n"
    "s_add_u32 s65, s65, s29\n"
    "s_lshl_b32 s65, s65, 13\n"
    "s_add_u32 s65, s65, s88\n"
    ".p2align 6\n"
    ".Lswin_LOOP_%=:\n"
    "s_waitcnt lgkmcnt(9)\n"
    "v_mfma_f32_16x16x32_bf16 v[154:157], a[72:75], v[138:141], 0\n"
    "s_waitcnt lgkmcnt(8)\n"
    "v_rcp_f32 v237, v236\n"
    "v_mfma_f32_16x16x32_bf16 v[154:157], a[76:79], v[100:103], v[154:157]\n"
    "s_waitcnt lgkmcnt(7)\n"
    "v_max3_f32 v255, v255, v236, v237\n"
    "v_mul_f32 v190, v237, v190\n"
    "v_mfma_f32_16x16x32_bf16 v[154:157], a[80:83], v[124:127], v[154:157]\n"
    "s_waitcnt lgkmcnt(6)\n"
    "v_mul_f32 v191, v237, v191\n"
    "v_cvt_pk_bf16_f32 v190, v190, v191\n"
    "v_mfma_f32_16x16x32_bf16 v[154:157], a[84:87], v[112:115], v[154:157]\n"
    "s_waitcnt lgkmcnt(5)\n"
    "v_mul_f32 v192, v237, v192\n"
    "v_mul_f32 v193, v237, v193\n"
    "v_mfma_f32_16x16x32_bf16 v[154:157], a[88:91], v[116:119], v[154:157]\n"
    "s_waitcnt lgkmcnt(0)\n"
    "v_cvt_pk_bf16_f32 v191, v192, v193\n"
    "v_mul_f32 v206, v237, v206\n"
    "v_mfma_f32_16x16x32_bf16 v[154:157], a[92:95], v[128:131], v[154:157]\n"
    "s_add_u32 s50, s97, 1953\n"
    "v_mul_f32 v207, v237, v207\n"
    "v_cvt_pk_bf16_f32 v192, v206, v207\n"
    "v_mfma_f32_16x16x32_bf16 v[104:107], a[96:99], v[138:141], 0\n"
    "s_mul_hi_u32 s48, s50, 91382283\n"
    "v_mul_f32 v208, v237, v208\n"
    "v_mul_f32 v209, v237, v209\n"
    "v_mfma_f32_16x16x32_bf16 v[104:107], a[100:103], v[100:103], v[104:107]\n"
    "s_mul_i32 s49, s48, 47\n"
    "v_cvt_pk_bf16_f32 v158, v154, v155\n"
    "v_cvt_pk_bf16_f32 v159, v156, v157\n"
    "v_mfma_f32_16x16x32_bf16 v[104:107], a[104:107], v[124:127], v[104:107]\n"
    "s_sub_u32 s49, s50, s49\n"
    "v_cvt_pk_bf16_f32 v193, v208, v209\n"
    "v_mul_f32 v182, v237, v182\n"
    "v_mfma_f32_16x16x32_bf16 v[104:107], a[108:111], v[112:115], v[104:107]\n"
    "s_sub_u32 s50, s97, 48\n"
    "v_mul_f32 v183, v237, v183\n"
    "v_cvt_pk_bf16_f32 v182, v182, v183\n"
    "v_mfma_f32_16x16x32_bf16 v[104:107], a[112:115], v[116:119], v[104:107]\n"
    "s_cmp_lt_u32 s97, 95\n"
    "s_cselect_b32 s48, s50, s48\n"
    "s_cselect_b32 s49, 47, s49\n"
    "v_mfma_f32_16x16x32_bf16 v[104:107], a[116:119], v[128:131], v[104:107]\n"
    "s_cmp_lt_u32 s97, 48\n"
    "s_cselect_b32 s48, 47, s48\n"
    "s_cselect_b32 s49, s97, s49\n"
    "v_mfma_f32_16x16x32_bf16 v[154:157], a[120:123], v[138:141], 0\n"
    "s_mul_i32 s68, s48, 1179648\n"
    "v_mul_f32 v184, v237, v184\n"
    "v_mul_f32 v185, v237, v185\n"
    "v_mfma_f32_16x16x32_bf16 v[154:157], a[124:127], v[100:103], v[154:157]\n"
    "s_mul_i32 s50, s49, 3072\n"
    "v_cvt_pk_bf16_f32 v160, v104, v105\n"
    "v_cvt_pk_bf16_f32 v161, v106, v107\n"
    "v_mfma_f32_16x16x32_bf16 v[154:157], a[128:131], v[124:127], v[154:157]\n"
    "s_add_u32 s68, s68, s50\n"
    "v_cvt_pk_bf16_f32 v183, v184, v185\n"
    "v_cvt_pk_bf16_f32 v108, v108, v109\n"
    "v_mfma_f32_16x16x32_bf16 v[154:157], a[132:135], v[112:115], v[154:157]\n"
    "s_add_u32 s68, s68, 591360\n"
    "v_rcp_f32 v133, v132\n"
    "v_mfma_f32_16x16x32_bf16 v[154:157], a[136:139], v[116:119], v[154:157]\n"
    "s_mul_i32 s69, s48, 48\n"
    "v_max3_f32 v255, v255, v132, v133\n"
    "v_mul_f32 v178, v133, v178\n"
    "v_mfma_f32_16x16x32_bf16 v[154:157], a[140:143], v[128:131], v[154:157]\n"
    "s_add_u32 s69, s69, s49\n"
    "v_mul_f32 v179, v133, v179\n"
    "v_cvt_pk_bf16_f32 v178, v178, v179\n"
    "v_mfma_f32_16x16x32_bf16 v[166:169], v[138:141], a[144:147], 0\n"
    "s_lshl_b32 s69, s69, 13\n"
    "v_mul_f32 v180, v133, v180\n"
    "v_mul_f32 v181, v133, v181\n"
    "v_mfma_f32_16x16x32_bf16 v[166:169], v[100:103], a[148:151], v[166:169]\n"
    "s_add_u32 s69, s69, s88\n"
    "v_cvt_pk_bf16_f32 v134, v154, v155\n"
    "v_cvt_pk_bf16_f32 v135, v156, v157\n"
    "v_mfma_f32_16x16x32_bf16 v[166:169], v[124:127], a[152:155], v[166:169]\n"
    "s_cmp_eq_u32 s49, 47\n"
    "s_cselect_b32 s25, 6, -1\n"
    "s_cselect_b32 s50, 145920, 0\n"
    "v_mfma_f32_16x16x32_bf16 v[166:169], v[112:115], a[156:159], v[166:169]\n"
    "s_sub_u32 s68, s68, s50\n"
    "v_cvt_pk_bf16_f32 v179, v180, v181\n"
    "v_mul_f32 v150, v133, v150\n"
    "v_mfma_f32_16x16x32_bf16 v[166:169], v[116:119], a[160:163], v[166:169]\n"
    "s_cmp_eq_u32 s48, 47\n"
    "s_cselect_b32 s50, 56623104, 0\n"
    "v_mul_f32 v151, v133, v151\n"
    "v_mfma_f32_16x16x32_bf16 v[166:169], v[128:131], a[164:167], v[166:169]\n"
    "s_cmp_ge_u32 s26, 2\n"
    "s_cselect_b32 s50, s50, 0\n"
    "v_cvt_pk_bf16_f32 v180, v150, v151\n"
    "v_mfma_f32_16x16x32_bf16 v[104:107], v[138:141], a[168:171], 0\n"
    "s_sub_u32 s68, s68, s50\n"
    "v_mul_f32 v152, v133, v152\n"
    "v_mul_f32 v153, v133, v153\n"
    "v_mfma_f32_16x16x32_bf16 v[104:107], v[100:103], a[172:175], v[104:107]\n"
    "s_cmp_eq_u32 s84, s25\n"
    "s_cbranch_scc1 .Lswin_PW_%=\n"
    ".Lswin_PWR_%=:\n"
    "v_cvt_pk_bf16_f32 v150, v166, v167\n"
    "v_mfma_f32_16x16x32_bf16 v[104:107], v[124:127], a[176:179], v[104:107]\n"
    "s_add_u32 s20, s64, s86\n"
    "v_cvt_pk_bf16_f32 v151, v168, v169\n"
    "v_cvt_pk_bf16_f32 v181, v152, v153\n"
    "v_mfma_f32_16x16x32_bf16 v[104:107], v[112:115], a[180:183], v[104:107]\n"
    "s_add_u32 m0, s98, 0\n"
    "v_mul_f32 v144, v133, v144\n"
    "v_mul_f32 v145, v133, v145\n"
    "v_mfma_f32_16x16x32_bf16 v[104:107], v[116:119], a[184:187], v[104:107]\n"
    "s_sub_u32 s96, 221184, s91\n"
    "v_cvt_pk_bf16_f32 v144, v144, v145\n"
    "v_mul_f32 v146, v133, v146\n"
    "v_mfma_f32_16x16x32_bf16 v[104:107], v[128:131], a[188:191], v[104:107]\n"
    "ds_write_b128 v246, v[190:193] offset:12288\n"
    "v_mfma_f32_16x16x32_bf16 v[166:169], v[138:141], a[192:195], 0\n"
    "s_sub_u32 s23, s96, s91\n"
    "v_mul_f32 v147, v133, v147\n"
    "v_cvt_pk_bf16_f32 v145, v146, v147\n"
    "v_mfma_f32_16x16x32_bf16 v[166:169], v[100:103], a[196:199], v[166:169]\n"
    "s_mov_b32 s91, s96\n"
    "v_cvt_pk_bf16_f32 v146, v104, v105\n"
    "v_cvt_pk_bf16_f32 v147, v106, v107\n"
    "v_mfma_f32_16x16x32_bf16 v[166:169], v[124:127], a[200:203], v[166:169]\n"
    "s_xor_b32 s93, s92, 8192\n"
    "v_cvt_pk_bf16_f32 v109, v110, v111\n"
    "v_cvt_pk_bf16_f32 v110, v162, v163\n"
    "v_mfma_f32_16x16x32_bf16 v[166:169], v[112:115], a[204:207], v[166:169]\n"
    "s_add_u32 s22, s56, s86\n"
    "v_cvt_pk_bf16_f32 v111, v164, v165\n"
    "v_cvt_pk_bf16_f32 v96, v96, v97\n"
    "v_mfma_f32_16x16x32_bf16 v[166:169], v[116:119], a[208:211], v[166:169]\n"
    "s_sub_u32 s24, s89, s90\n"
    "v_cvt_pk_bf16_f32 v97, v98, v99\n"
    "v_add_u32 v243, s92, v250\n"
    "v_mfma_f32_16x16x32_bf16 v[166:169], v[128:131], a[212:215], v[166:169]\n"
    "ds_write_b64 v252, v[182:183] offset:12288\n"
    "v_mfma_f32_16x16x32_bf16 v[162:165], a[0:3], v[138:141], 0\n"
    "v_mfma_f32_16x16x32_bf16 v[162:165], a[4:7], v[100:103], v[162:165]\n"
    "v_cvt_pk_bf16_f32 v154, v166, v167\n"
    "v_cvt_pk_bf16_f32 v155, v168, v169\n"
    "v_mfma_f32_16x16x32_bf16 v[162:165], a[8:11], v[124:127], v[162:165]\n"
    "v_mfma_f32_16x16x32_bf16 v[162:165], a[12:15], v[112:115], v[162:165]\n"
    "v_mfma_f32_16x16x32_bf16 v[162:165], a[16:19], v[116:119], v[162:165]\n"
    "ds_write_b128 v246, v[178:181] offset:18432\n"
    "v_mfma_f32_16x16x32_bf16 v[162:165], a[20:23], v[128:131], v[162:165]\n"
    "ds_write_b64 v252, v[144:145] offset:18432\n"
    "v_mfma_f32_16x16x32_bf16 v[190:193], a[24:27], v[138:141], 0\n"
    "ds_write_b128 v245, v[108:111] offset:18432\n"
    "v_mfma_f32_16x16x32_bf16 v[190:193], a[28:31], v[100:103], v[190:193]\n"
    "ds_write_b64 v253, v[96:97] offset:18432\n"
    "v_mfma_f32_16x16x32_bf16 v[190:193], a[32:35], v[124:127], v[190:193]\n"
    "ds_read_b128 v[178:181], v254 offset:6144\n"
    "v_mul_f32 v162, s82, v162\n"
    "v_mfma_f32_16x16x32_bf16 v[190:193], a[36:39], v[112:115], v[190:193]\n"
    "ds_read_b128 v[166:169], v248 offset:6144\n"
    "v_mul_f32 v163, s82, v163\n"
    "v_mfma_f32_16x16x32_bf16 v[190:193], a[40:43], v[116:119], v[190:193]\n"
    "ds_read_b128 v[174:177], v254 offset:6272\n"
    "v_mul_f32 v164, s82, v164\n"
    "v_mfma_f32_16x16x32_bf16 v[190:193], a[44:47], v[128:131], v[190:193]\n"
    "ds_read_b128 v[104:107], v248 offset:6272\n"
    "v_mul_f32 v165, s82, v165\n"
    "v_mfma_f32_16x16x32_bf16 v[182:185], a[48:51], v[138:141], 0\n"
    "ds_read_b128 v[170:173], v254 offset:6400\n"
    "v_cvt_pk_bf16_f32 v96, v162, v163\n"
    "v_mfma_f32_16x16x32_bf16 v[182:185], a[52:55], v[100:103], v[182:185]\n"
    "ds_read_b128 v[108:111], v248 offset:6400\n"
    "v_cvt_pk_bf16_f32 v97, v164, v165\n"
    "v_mfma_f32_16x16x32_bf16 v[182:185], a[56:59], v[124:127], v[182:185]\n"
    "buffer_load_dwordx4 v251, s[36:39], s20 offen offset:0 lds\n"
    "v_mfma_f32_16x16x32_bf16 v[182:185], a[60:63], v[112:115], v[182:185]\n"
    "v_mul_f32 v190, s82, v190\n"
    "v_mul_f32 v191, s82, v191\n"
    "v_mfma_f32_16x16x32_bf16 v[182:185], a[64:67], v[116:119], v[182:185]\n"
    "v_mul_f32 v192, s82, v192\n"
    "v_mul_f32 v193, s82, v193\n"
    "v_mfma_f32_16x16x32_bf16 v[182:185], a[68:71], v[128:131], v[182:185]\n"
    "s_waitcnt lgkmcnt(3)\n"
    "v_cvt_pk_bf16_f32 v98, v190, v191\n"
    "v_cvt_pk_bf16_f32 v99, v192, v193\n"
    "v_mfma_f32_16x16x32_bf16 v[112:115], a[72:75], v[178:181], 0\n"
    "v_add_u32 v246, s23, v246\n"
    "v_add_u32 v252, s23, v252\n"
    "v_mfma_f32_16x16x32_bf16 v[112:115], a[76:79], v[166:169], v[112:115]\n"
    "v_mul_f32 v182, s82, v182\n"
    "v_mul_f32 v183, s82, v183\n"
    "v_mfma_f32_16x16x32_bf16 v[112:115], a[80:83], v[174:177], v[112:115]\n"
    "s_waitcnt lgkmcnt(0)\n"
    "v_mul_f32 v184, s82, v184\n"
    "v_mul_f32 v185, s82, v185\n"
    "v_mfma_f32_16x16x32_bf16 v[112:115], a[84:87], v[104:107], v[112:115]\n"
    "v_cvt_pk_bf16_f32 v100, v182, v183\n"
    "v_cvt_pk_bf16_f32 v101, v184, v185\n"
    "v_mfma_f32_16x16x32_bf16 v[112:115], a[88:91], v[170:173], v[112:115]\n"
    "v_add_u32 v245, s24, v245\n"
    "v_add_u32 v253, s24, v253\n"
    "v_mfma_f32_16x16x32_bf16 v[112:115], a[92:95], v[108:111], v[112:115]\n"
    "s_sub_u32 s23, 221184, s91\n"
    "s_sub_u32 s23, s23, s89\n"
    "v_mfma_f32_16x16x32_bf16 v[116:119], a[96:99], v[178:181], 0\n"
    "v_mfma_f32_16x16x32_bf16 v[116:119], a[100:103], v[166:169], v[116:119]\n"
    "v_cvt_pk_bf16_f32 v162, v112, v113\n"
    "v_cvt_pk_bf16_f32 v163, v114, v115\n"
    "v_mfma_f32_16x16x32_bf16 v[116:119], a[104:107], v[174:177], v[116:119]\n"
    "v_mfma_f32_16x16x32_bf16 v[116:119], a[108:111], v[104:107], v[116:119]\n"
    "v_mfma_f32_16x16x32_bf16 v[116:119], a[112:115], v[170:173], v[116:119]\n"
    "v_mfma_f32_16x16x32_bf16 v[116:119], a[116:119], v[108:111], v[116:119]\n"
    "v_mfma_f32_16x16x32_bf16 v[112:115], a[120:123], v[178:181], 0\n"
    "buffer_load_dwordx4 v249, s[36:39], s20 offen offset:1024 lds\n"
    "v_mfma_f32_16x16x32_bf16 v[112:115], a[124:127], v[166:169], v[112:115]\n"
    "v_cvt_pk_bf16_f32 v164, v116, v117\n"
    "v_cvt_pk_bf16_f32 v165, v118, v119\n"
    "v_mfma_f32_16x16x32_bf16 v[112:115], a[128:131], v[174:177], v[112:115]\n"
    "v_mfma_f32_16x16x32_bf16 v[112:115], a[132:135], v[104:107], v[112:115]\n"
    "v_mfma_f32_16x16x32_bf16 v[112:115], a[136:139], v[170:173], v[112:115]\n"
    "v_mfma_f32_16x16x32_bf16 v[112:115], a[140:143], v[108:111], v[112:115]\n"
    "v_mfma_f32_16x16x32_bf16 v[128:131], v[178:181], a[144:147], 0\n"
    "v_mfma_f32_16x16x32_bf16 v[128:131], v[166:169], a[148:151], v[128:131]\n"
    "v_cvt_pk_bf16_f32 v138, v112, v113\n"
    "v_cvt_pk_bf16_f32 v139, v114, v115\n"
    "v_mfma_f32_16x16x32_bf16 v[128:131], v[174:177], a[152:155], v[128:131]\n"
    "v_mfma_f32_16x16x32_bf16 v[128:131], v[104:107], a[156:159], v[128:131]\n"
    "v_mfma_f32_16x16x32_bf16 v[128:131], v[170:173], a[160:163], v[128:131]\n"
    "v_mfma_f32_16x16x32_bf16 v[128:131], v[108:111], a[164:167], v[128:131]\n"
    "v_mfma_f32_16x16x32_bf16 v[124:127], v[178:181], a[168:171], 0\n"
    "v_mfma_f32_16x16x32_bf16 v[124:127], v[166:169], a[172:175], v[124:127]\n"
    "v_cvt_pk_bf16_f32 v152, v128, v129\n"
    "v_cvt_pk_bf16_f32 v153, v130, v131\n"
    "v_mfma_f32_16x16x32_bf16 v[124:127], v[174:177], a[176:179], v[124:127]\n"
    "v_mfma_f32_16x16x32_bf16 v[124:127], v[104:107], a[180:183], v[124:127]\n"
    "v_mfma_f32_16x16x32_bf16 v[124:127], v[170:173], a[184:187], v[124:127]\n"
    "v_mfma_f32_16x16x32_bf16 v[124:127], v[108:111], a[188:191], v[124:127]\n"
    "v_mfma_f32_16x16x32_bf16 v[116:119], v[178:181], a[192:195], 0\n"
    "v_mfma_f32_16x16x32_bf16 v[116:119], v[166:169], a[196:199], v[116:119]\n"
    "v_cvt_pk_bf16_f32 v148, v124, v125\n"
    "v_cvt_pk_bf16_f32 v149, v126, v127\n"
    "v_mfma_f32_16x16x32_bf16 v[116:119], v[174:177], a[200:203], v[116:119]\n"
    "buffer_load_dwordx4 v247, s[36:39], s20 offen offset:2048 lds\n"
    "v_mfma_f32_16x16x32_bf16 v[116:119], v[104:107], a[204:207], v[116:119]\n"
    "s_add_u32 s20, s20, 147456\n"
    "s_add_u32 m0, s98, 3072\n"
    "s_add_u32 s98, s90, s87\n"
    "v_mfma_f32_16x16x32_bf16 v[116:119], v[170:173], a[208:211], v[116:119]\n"
    "s_mov_b32 s90, s89\n"
    "v_add_u32 v244, s98, v250\n"
    "s_add_u32 s89, s89, 24576\n"
    "v_mfma_f32_16x16x32_bf16 v[116:119], v[108:111], a[212:215], v[116:119]\n"
    "s_cmp_eq_u32 s89, 98304\n"
    "s_cselect_b32 s89, 0, s89\n"
    "s_add_u32 s95, s89, s91\n"
    "v_mfma_f32_16x16x32_bf16 v[112:115], a[0:3], v[178:181], 0\n"
    "s_sub_u32 s95, s95, 221184\n"
    "v_mfma_f32_16x16x32_bf16 v[112:115], a[4:7], v[166:169], v[112:115]\n"
    "v_cvt_pk_bf16_f32 v156, v116, v117\n"
    "v_cvt_pk_bf16_f32 v157, v118, v119\n"
    "v_mfma_f32_16x16x32_bf16 v[112:115], a[8:11], v[174:177], v[112:115]\n"
    "v_mfma_f32_16x16x32_bf16 v[112:115], a[12:15], v[104:107], v[112:115]\n"
    "v_mfma_f32_16x16x32_bf16 v[112:115], a[16:19], v[170:173], v[112:115]\n"
    "v_mfma_f32_16x16x32_bf16 v[112:115], a[20:23], v[108:111], v[112:115]\n"
    "v_mfma_f32_16x16x32_bf16 v[116:119], a[24:27], v[178:181], 0\n"
    "v_mfma_f32_16x16x32_bf16 v[116:119], a[28:31], v[166:169], v[116:119]\n"
    "v_mul_f32 v112, s82, v112\n"
    "v_mul_f32 v113, s82, v113\n"
    "v_mfma_f32_16x16x32_bf16 v[116:119], a[32:35], v[174:177], v[116:119]\n"
    "ds_read_b128 v[182:185], v254 offset:12288\n"
    "v_mul_f32 v114, s82, v114\n"
    "v_mfma_f32_16x16x32_bf16 v[116:119], a[36:39], v[104:107], v[116:119]\n"
    "ds_read_b128 v[190:193], v248 offset:12288\n"
    "v_mul_f32 v115, s82, v115\n"
    "v_mfma_f32_16x16x32_bf16 v[116:119], a[40:43], v[170:173], v[116:119]\n"
    "ds_read_b128 v[202:205], v254 offset:12416\n"
    "v_cvt_pk_bf16_f32 v102, v112, v113\n"
    "v_mfma_f32_16x16x32_bf16 v[116:119], a[44:47], v[108:111], v[116:119]\n"
    "ds_read_b128 v[126:129], v248 offset:12416\n"
    "v_cvt_pk_bf16_f32 v103, v114, v115\n"
    "v_mfma_f32_16x16x32_bf16 v[206:209], a[48:51], v[178:181], 0\n"
    "ds_read_b128 v[130:133], v254 offset:12544\n"
    "v_mfma_f32_16x16x32_bf16 v[206:209], a[52:55], v[166:169], v[206:209]\n"
    "ds_read_b128 v[112:115], v248 offset:12544\n"
    "v_mul_f32 v116, s82, v116\n"
    "v_mfma_f32_16x16x32_bf16 v[206:209], a[56:59], v[174:177], v[206:209]\n"
    "v_mul_f32 v117, s82, v117\n"
    "v_mul_f32 v118, s82, v118\n"
    "v_mfma_f32_16x16x32_bf16 v[206:209], a[60:63], v[104:107], v[206:209]\n"
    "v_mul_f32 v119, s82, v119\n"
    "v_cvt_pk_bf16_f32 v104, v116, v117\n"
    "v_mfma_f32_16x16x32_bf16 v[206:209], a[64:67], v[170:173], v[206:209]\n"
    "buffer_load_dwordx4 v251, s[36:39], s20 offen offset:0 lds\n"
    "v_mfma_f32_16x16x32_bf16 v[206:209], a[68:71], v[108:111], v[206:209]\n"
    "s_waitcnt lgkmcnt(3)\n"
    "v_cvt_pk_bf16_f32 v105, v118, v119\n"
    "v_mfma_f32_16x16x32_bf16 v[108:111], a[72:75], v[182:185], 0\n"
    "v_mfma_f32_16x16x32_bf16 v[108:111], a[76:79], v[190:193], v[108:111]\n"
    "v_mul_f32 v206, s82, v206\n"
    "v_mul_f32 v207, s82, v207\n"
    "v_mfma_f32_16x16x32_bf16 v[108:111], a[80:83], v[202:205], v[108:111]\n"
    "s_waitcnt lgkmcnt(0)\n"
    "v_mul_f32 v208, s82, v208\n"
    "v_mul_f32 v209, s82, v209\n"
    "v_mfma_f32_16x16x32_bf16 v[108:111], a[84:87], v[126:129], v[108:111]\n"
    "v_cvt_pk_bf16_f32 v106, v206, v207\n"
    "v_cvt_pk_bf16_f32 v107, v208, v209\n"
    "v_mfma_f32_16x16x32_bf16 v[108:111], a[88:91], v[130:133], v[108:111]\n"
    "v_mfma_f32_16x16x32_bf16 v[108:111], a[92:95], v[112:115], v[108:111]\n"
    "v_mfma_f32_16x16x32_bf16 v[166:169], a[96:99], v[182:185], 0\n"
    "v_mfma_f32_16x16x32_bf16 v[166:169], a[100:103], v[190:193], v[166:169]\n"
    "v_cvt_pk_bf16_f32 v178, v108, v109\n"
    "v_cvt_pk_bf16_f32 v179, v110, v111\n"
    "v_mfma_f32_16x16x32_bf16 v[166:169], a[104:107], v[202:205], v[166:169]\n"
    "v_mfma_f32_16x16x32_bf16 v[166:169], a[108:111], v[126:129], v[166:169]\n"
    "v_mfma_f32_16x16x32_bf16 v[166:169], a[112:115], v[130:133], v[166:169]\n"
    "v_mfma_f32_16x16x32_bf16 v[166:169], a[116:119], v[112:115], v[166:169]\n"
    "v_mfma_f32_16x16x32_bf16 v[116:119], a[120:123], v[182:185], 0\n"
    "v_mfma_f32_16x16x32_bf16 v[116:119], a[124:127], v[190:193], v[116:119]\n"
    "v_cvt_pk_bf16_f32 v180, v166, v167\n"
    "v_cvt_pk_bf16_f32 v181, v168, v169\n"
    "v_mfma_f32_16x16x32_bf16 v[116:119], a[128:131], v[202:205], v[116:119]\n"
    "v_mfma_f32_16x16x32_bf16 v[116:119], a[132:135], v[126:129], v[116:119]\n"
    "v_mfma_f32_16x16x32_bf16 v[116:119], a[136:139], v[130:133], v[116:119]\n"
    "v_mfma_f32_16x16x32_bf16 v[116:119], a[140:143], v[112:115], v[116:119]\n"
    "v_mfma_f32_16x16x32_bf16 v[108:111], v[182:185], a[144:147], 0\n"
    "buffer_load_dwordx4 v249, s[36:39], s20 offen offset:1024 lds\n"
    "v_mfma_f32_16x16x32_bf16 v[108:111], v[190:193], a[148:151], v[108:111]\n"
    "v_cvt_pk_bf16_f32 v140, v116, v117\n"
    "v_cvt_pk_bf16_f32 v141, v118, v119\n"
    "v_mfma_f32_16x16x32_bf16 v[108:111], v[202:205], a[152:155], v[108:111]\n"
    "v_mfma_f32_16x16x32_bf16 v[108:111], v[126:129], a[156:159], v[108:111]\n"
    "v_mfma_f32_16x16x32_bf16 v[108:111], v[130:133], a[160:163], v[108:111]\n"
    "v_mfma_f32_16x16x32_bf16 v[108:111], v[112:115], a[164:167], v[108:111]\n"
    "v_mfma_f32_16x16x32_bf16 v[172:175], v[182:185], a[168:171], 0\n"
    "v_mfma_f32_16x16x32_bf16 v[172:175], v[190:193], a[172:175], v[172:175]\n"
    "v_cvt_pk_bf16_f32 v170, v108, v109\n"
    "v_cvt_pk_bf16_f32 v171, v110, v111\n"
    "v_mfma_f32_16x16x32_bf16 v[172:175], v[202:205], a[176:179], v[172:175]\n"
    "v_mfma_f32_16x16x32_bf16 v[172:175], v[126:129], a[180:183], v[172:175]\n"
    "v_mfma_f32_16x16x32_bf16 v[172:175], v[130:133], a[184:187], v[172:175]\n"
    "v_mfma_f32_16x16x32_bf16 v[172:175], v[112:115], a[188:191], v[172:175]\n"
    "v_mfma_f32_16x16x32_bf16 v[116:119], v[182:185], a[192:195], 0\n"
    "v_mfma_f32_16x16x32_bf16 v[116:119], v[190:193], a[196:199], v[116:119]\n"
    "v_cvt_pk_bf16_f32 v166, v172, v173\n"
    "v_cvt_pk_bf16_f32 v167, v174, v175\n"
    "v_mfma_f32_16x16x32_bf16 v[116:119], v[202:205], a[200:203], v[116:119]\n"
    "v_mfma_f32_16x16x32_bf16 v[116:119], v[126:129], a[204:207], v[116:119]\n"
    "v_mfma_f32_16x16x32_bf16 v[116:119], v[130:133], a[208:211], v[116:119]\n"
    "v_mfma_f32_16x16x32_bf16 v[116:119], v[112:115], a[212:215], v[116:119]\n"
    "v_mfma_f32_16x16x32_bf16 v[186:189], a[0:3], v[182:185], 0\n"
    "v_mfma_f32_16x16x32_bf16 v[186:189], a[4:7], v[190:193], v[186:189]\n"
    "v_cvt_pk_bf16_f32 v174, v116, v117\n"
    "v_cvt_pk_bf16_f32 v175, v118, v119\n"
    "v_mfma_f32_16x16x32_bf16 v[186:189], a[8:11], v[202:205], v[186:189]\n"
    "buffer_load_dwordx4 v247, s[36:39], s20 offen offset:2048 lds\n"
    "v_mfma_f32_16x16x32_bf16 v[186:189], a[12:15], v[126:129], v[186:189]\n"
    "s_cmp_eq_u32 s84, s25\n"
    "s_cbranch_scc1 .Lswin_PV_%=\n"
    ".Lswin_PVR_%=:\n"
    "s_add_u32 m0, s93, s88\n"
    "v_mfma_f32_16x16x32_bf16 v[186:189], a[16:19], v[130:133], v[186:189]\n"
    "v_mfma_f32_16x16x32_bf16 v[186:189], a[20:23], v[112:115], v[186:189]\n"
    "v_mfma_f32_16x16x32_bf16 v[226:229], a[24:27], v[182:185], 0\n"
    "v_mfma_f32_16x16x32_bf16 v[226:229], a[28:31], v[190:193], v[226:229]\n"
    "v_mul_f32 v186, s82, v186\n"
    "v_mul_f32 v187, s82, v187\n"
    "v_mfma_f32_16x16x32_bf16 v[226:229], a[32:35], v[202:205], v[226:229]\n"
    "ds_read_b128 v[198:201], v254 offset:18432\n"
    "v_mul_f32 v188, s82, v188\n"
    "v_mfma_f32_16x16x32_bf16 v[226:229], a[36:39], v[126:129], v[226:229]\n"
    "ds_read_b128 v[116:119], v248 offset:18432\n"
    "v_mul_f32 v189, s82, v189\n"
    "v_mfma_f32_16x16x32_bf16 v[226:229], a[40:43], v[130:133], v[226:229]\n"
    "ds_read_b128 v[194:197], v254 offset:18560\n"
    "v_cvt_pk_bf16_f32 v108, v186, v187\n"
    "v_mfma_f32_16x16x32_bf16 v[226:229], a[44:47], v[112:115], v[226:229]\n"
    "ds_read_b128 v[206:209], v248 offset:18560\n"
    "v_cvt_pk_bf16_f32 v109, v188, v189\n"
    "v_mfma_f32_16x16x32_bf16 v[214:217], a[48:51], v[182:185], 0\n"
    "ds_read_b128 v[186:189], v254 offset:18688\n"
    "v_add_u32 v254, s23, v254\n"
    "v_mfma_f32_16x16x32_bf16 v[214:217], a[52:55], v[190:193], v[214:217]\n"
    "ds_read_b128 v[190:193], v248 offset:18688\n"
    "v_add_u32 v248, s23, v248\n"
    "v_mfma_f32_16x16x32_bf16 v[214:217], a[56:59], v[202:205], v[214:217]\n"
    "v_mul_f32 v226, s82, v226\n"
    "v_mul_f32 v227, s82, v227\n"
    "v_mfma_f32_16x16x32_bf16 v[214:217], a[60:63], v[126:129], v[214:217]\n"
    "v_mul_f32 v228, s82, v228\n"
    "v_mul_f32 v229, s82, v229\n"
    "v_mfma_f32_16x16x32_bf16 v[214:217], a[64:67], v[130:133], v[214:217]\n"
    "v_cvt_pk_bf16_f32 v110, v226, v227\n"
    "v_cvt_pk_bf16_f32 v111, v228, v229\n"
    "v_mfma_f32_16x16x32_bf16 v[214:217], a[68:71], v[112:115], v[214:217]\n"
    "s_waitcnt lgkmcnt(3)\n"
    "v_mfma_f32_16x16x32_bf16 v[202:205], a[72:75], v[198:201], 0\n"
    "v_mfma_f32_16x16x32_bf16 v[202:205], a[76:79], v[116:119], v[202:205]\n"
    "v_mul_f32 v214, s82, v214\n"
    "v_mul_f32 v215, s82, v215\n"
    "v_mfma_f32_16x16x32_bf16 v[202:205], a[80:83], v[194:197], v[202:205]\n"
    "s_waitcnt lgkmcnt(0)\n"
    "v_mul_f32 v216, s82, v216\n"
    "v_mul_f32 v217, s82, v217\n"
    "v_mfma_f32_16x16x32_bf16 v[202:205], a[84:87], v[206:209], v[202:205]\n"
    "v_cvt_pk_bf16_f32 v112, v214, v215\n"
    "v_cvt_pk_bf16_f32 v113, v216, v217\n"
    "v_mfma_f32_16x16x32_bf16 v[202:205], a[88:91], v[186:189], v[202:205]\n"
    "s_waitcnt vmcnt(12) lgkmcnt(0)\n"
    "v_mfma_f32_16x16x32_bf16 v[202:205], a[92:95], v[190:193], v[202:205]\n"
    "s_barrier\n"
    "v_mfma_f32_16x16x32_bf16 v[128:131], a[96:99], v[198:201], 0\n"
    "ds_read_b128 v[214:217], v243 offset:0\n"
    "v_mfma_f32_16x16x32_bf16 v[128:131], a[100:103], v[116:119], v[128:131]\n"
    "buffer_load_dwordx4 v250, s[44:47], s63 offen offset:0 lds\n"
    "v_mfma_f32_16x16x32_bf16 v[128:131], a[104:107], v[194:197], v[128:131]\n"
    "ds_read_b128 v[124:127], v244 offset:0\n"
    "v_cvt_pk_bf16_f32 v182, v202, v203\n"
    "v_mfma_f32_16x16x32_bf16 v[128:131], a[108:111], v[206:209], v[128:131]\n"
    "ds_read_b128 v[226:229], v243 offset:1024\n"
    "v_cvt_pk_bf16_f32 v183, v204, v205\n"
    "v_mfma_f32_16x16x32_bf16 v[128:131], a[112:115], v[186:189], v[128:131]\n"
    "v_mfma_f32_16x16x32_bf16 v[128:131], a[116:119], v[190:193], v[128:131]\n"
    "buffer_load_dwordx4 v250, s[44:47], s63 offen offset:1024 lds\n"
    "v_mfma_f32_16x16x32_bf16 v[202:205], a[120:123], v[198:201], 0\n"
    "ds_read_b128 v[210:213], v243 offset:2048\n"
    "v_mfma_f32_16x16x32_bf16 v[202:205], a[124:127], v[116:119], v[202:205]\n"
    "v_cvt_pk_bf16_f32 v184, v128, v129\n"
    "v_cvt_pk_bf16_f32 v185, v130, v131\n"
    "v_mfma_f32_16x16x32_bf16 v[202:205], a[128:131], v[194:197], v[202:205]\n"
    "s_waitcnt lgkmcnt(3)\n"
    "v_or3_b32 v242, v214, v215, v216\n"
    "v_or_b32 v242, v242, v217\n"
    "v_mfma_f32_16x16x32_bf16 v[202:205], a[132:135], v[206:209], v[202:205]\n"
    "ds_read_b128 v[214:217], v243 offset:3072\n"
    "v_mfma_f32_16x16x32_bf16 v[202:205], a[136:139], v[186:189], v[202:205]\n"
    "v_mfma_f32_16x16x32_bf16 v[202:205], a[140:143], v[190:193], v[202:205]\n"
    "s_waitcnt lgkmcnt(2)\n"
    "v_or3_b32 v242, v242, v226, v227\n"
    "v_or3_b32 v242, v242, v228, v229\n"
    "v_mfma_f32_16x16x32_bf16 v[226:229], v[198:201], a[144:147], 0\n"
    "ds_read_b128 v[236:239], v243 offset:4096\n"
    "v_mfma_f32_16x16x32_bf16 v[226:229], v[116:119], a[148:151], v[226:229]\n"
    "buffer_store_dwordx4 v[124:127], v251, s[40:43], s22 offen offset:0 sc1\n"
    "v_mfma_f32_16x16x32_bf16 v[226:229], v[194:197], a[152:155], v[226:229]\n"
    "ds_read_b128 v[128:131], v244 offset:1024\n"
    "s_waitcnt lgkmcnt(3)\n"
    "v_or3_b32 v242, v242, v210, v211\n"
    "v_mfma_f32_16x16x32_bf16 v[226:229], v[206:209], a[156:159], v[226:229]\n"
    "ds_read_b128 v[124:127], v243 offset:5120\n"
    "v_or3_b32 v242, v242, v212, v213\n"
    "v_mfma_f32_16x16x32_bf16 v[226:229], v[186:189], a[160:163], v[226:229]\n"
    "s_waitcnt lgkmcnt(3)\n"
    "v_or3_b32 v242, v242, v214, v215\n"
    "v_or3_b32 v242, v242, v216, v217\n"
    "v_mfma_f32_16x16x32_bf16 v[226:229], v[190:193], a[164:167], v[226:229]\n"
    "v_cvt_pk_bf16_f32 v144, v202, v203\n"
    "v_cvt_pk_bf16_f32 v145, v204, v205\n"
    "v_mfma_f32_16x16x32_bf16 v[214:217], v[198:201], a[168:171], 0\n"
    "ds_read_b128 v[210:213], v243 offset:6144\n"
    "v_mfma_f32_16x16x32_bf16 v[214:217], v[116:119], a[172:175], v[214:217]\n"
    "s_waitcnt lgkmcnt(3)\n"
    "v_or3_b32 v242, v242, v236, v237\n"
    "v_or3_b32 v242, v242, v238, v239\n"
    "v_mfma_f32_16x16x32_bf16 v[214:217], v[194:197], a[176:179], v[214:217]\n"
    "v_cvt_pk_bf16_f32 v172, v226, v227\n"
    "v_cvt_pk_bf16_f32 v173, v228, v229\n"
    "v_mfma_f32_16x16x32_bf16 v[214:217], v[206:209], a[180:183], v[214:217]\n"
    "ds_read_b128 v[202:205], v243 offset:7168\n"
    "v_mfma_f32_16x16x32_bf16 v[214:217], v[186:189], a[184:187], v[214:217]\n"
    "s_waitcnt lgkmcnt(2)\n"
    "buffer_store_dwordx4 v[128:131], v249, s[40:43], s22 offen offset:1024 sc1\n"
    "v_mfma_f32_16x16x32_bf16 v[214:217], v[190:193], a[188:191], v[214:217]\n"
    "ds_read_b128 v[226:229], v244 offset:2048\n"
    "v_or3_b32 v242, v242, v124, v125\n"
    "v_mfma_f32_16x16x32_bf16 v[236:239], v[198:201], a[192:195], 0\n"
    "v_or3_b32 v242, v242, v126, v127\n"
    "v_mfma_f32_16x16x32_bf16 v[236:239], v[116:119], a[196:199], v[236:239]\n"
    "s_waitcnt lgkmcnt(2)\n"
    "v_or3_b32 v242, v242, v210, v211\n"
    "v_or3_b32 v242, v242, v212, v213\n"
    "v_mfma_f32_16x16x32_bf16 v[236:239], v[194:197], a[200:203], v[236:239]\n"
    "v_cvt_pk_bf16_f32 v168, v214, v215\n"
    "v_cvt_pk_bf16_f32 v169, v216, v217\n"
    "v_mfma_f32_16x16x32_bf16 v[236:239], v[206:209], a[204:207], v[236:239]\n"
    "v_mfma_f32_16x16x32_bf16 v[236:239], v[186:189], a[208:211], v[236:239]\n"
    "s_waitcnt lgkmcnt(1)\n"
    "v_or3_b32 v242, v242, v202, v203\n"
    "v_or3_b32 v242, v242, v204, v205\n"
    "v_mfma_f32_16x16x32_bf16 v[236:239], v[190:193], a[212:215], v[236:239]\n"
    "v_cmp_ne_u32_e64 s[76:77], 0, v242\n"
    "v_mfma_f32_16x16x32_bf16 v[202:205], a[0:3], v[198:201], 0\n"
    "s_mov_b64 vcc, s[76:77]\n"
    "s_cbranch_vccnz .Lswin_P1_%=\n"
    ".Lswin_P1R_%=:\n"
    "v_mfma_f32_16x16x32_bf16 v[202:205], a[4:7], v[116:119], v[202:205]\n"
    "v_cvt_pk_bf16_f32 v176, v236, v237\n"
    "v_cvt_pk_bf16_f32 v177, v238, v239\n"
    "v_mfma_f32_16x16x32_bf16 v[202:205], a[8:11], v[194:197], v[202:205]\n"
    "s_waitcnt lgkmcnt(0)\n"
    "buffer_store_dwordx4 v[226:229], v247, s[40:43], s22 offen offset:2048 sc1\n"
    "v_mfma_f32_16x16x32_bf16 v[202:205], a[12:15], v[206:209], v[202:205]\n"
    "s_add_u32 s22, s22, 147456\n"
    "ds_read_b128 v[128:131], v244 offset:3072\n"
    "v_mfma_f32_16x16x32_bf16 v[202:205], a[16:19], v[186:189], v[202:205]\n"
    "v_mfma_f32_16x16x32_bf16 v[202:205], a[20:23], v[190:193], v[202:205]\n"
    "v_mfma_f32_16x16x32_bf16 v[124:127], a[24:27], v[198:201], 0\n"
    "v_mfma_f32_16x16x32_bf16 v[124:127], a[28:31], v[116:119], v[124:127]\n"
    "v_mul_f32 v202, s82, v202\n"
    "v_mul_f32 v203, s82, v203\n"
    "v_mfma_f32_16x16x32_bf16 v[124:127], a[32:35], v[194:197], v[124:127]\n"
    "v_mul_f32 v204, s82, v204\n"
    "v_mul_f32 v205, s82, v205\n"
    "v_mfma_f32_16x16x32_bf16 v[124:127], a[36:39], v[206:209], v[124:127]\n"
    "v_cvt_pk_bf16_f32 v114, v202, v203\n"
    "v_cvt_pk_bf16_f32 v115, v204, v205\n"
    "v_mfma_f32_16x16x32_bf16 v[124:127], a[40:43], v[186:189], v[124:127]\n"
    "v_mfma_f32_16x16x32_bf16 v[124:127], a[44:47], v[190:193], v[124:127]\n"
    "v_mfma_f32_16x16x32_bf16 v[202:205], a[48:51], v[198:201], 0\n"
    "s_waitcnt lgkmcnt(0)\n"
    "buffer_store_dwordx4 v[128:131], v251, s[40:43], s22 offen offset:0 sc1\n"
    "v_mfma_f32_16x16x32_bf16 v[202:205], a[52:55], v[116:119], v[202:205]\n"
    "v_mul_f32 v124, s82, v124\n"
    "v_mul_f32 v125, s82, v125\n"
    "v_mfma_f32_16x16x32_bf16 v[202:205], a[56:59], v[194:197], v[202:205]\n"
    "v_mul_f32 v126, s82, v126\n"
    "v_mul_f32 v127, s82, v127\n"
    "v_mfma_f32_16x16x32_bf16 v[202:205], a[60:63], v[206:209], v[202:205]\n"
    "v_cvt_pk_bf16_f32 v116, v124, v125\n"
    "v_cvt_pk_bf16_f32 v117, v126, v127\n"
    "v_mfma_f32_16x16x32_bf16 v[202:205], a[64:67], v[186:189], v[202:205]\n"
    "ds_read_b128 v[224:227], v254 offset:0\n"
    "v_mfma_f32_16x16x32_bf16 v[202:205], a[68:71], v[190:193], v[202:205]\n"
    "ds_read_b128 v[218:221], v248 offset:0\n"
    "v_mfma_f32_16x16x32_bf16 v[126:129], v[158:161], v[96:99], v[0:3]\n"
    "ds_read_b128 v[232:235], v254 offset:128\n"
    "v_mfma_f32_16x16x32_bf16 v[126:129], v[134:137], v[100:103], v[126:129]\n"
    "ds_read_b128 v[214:217], v248 offset:128\n"
    "v_mul_f32 v202, s82, v202\n"
    "v_mfma_f32_16x16x32_bf16 v[190:193], v[162:165], v[96:99], v[4:7]\n"
    "ds_read_b128 v[228:231], v254 offset:256\n"
    "v_mul_f32 v203, s82, v203\n"
    "v_mfma_f32_16x16x32_bf16 v[190:193], v[136:139], v[98:101], v[190:193]\n"
    "ds_read_b128 v[198:201], v248 offset:256\n"
    "v_mul_f32 v204, s82, v204\n"
    "v_mfma_f32_16x16x32_bf16 v[194:197], v[178:181], v[96:99], v[8:11]\n"
    "v_exp_f32 v126, v126\n"
    "v_mfma_f32_16x16x32_bf16 v[194:197], v[140:143], v[100:103], v[194:197]\n"
    "v_exp_f32 v127, v127\n"
    "v_mfma_f32_16x16x32_bf16 v[236:239], v[182:185], v[96:99], v[12:15]\n"
    "v_exp_f32 v128, v128\n"
    "v_mfma_f32_16x16x32_bf16 v[236:239], v[142:145], v[98:101], v[236:239]\n"
    "s_waitcnt lgkmcnt(3)\n"
    "v_exp_f32 v194, v194\n"
    "v_mfma_f32_16x16x32_bf16 v[206:209], a[216:219], v[224:227], 0\n"
    "v_exp_f32 v195, v195\n"
    "v_mfma_f32_16x16x32_bf16 v[206:209], a[220:223], v[218:221], v[206:209]\n"
    "v_exp_f32 v129, v129\n"
    "v_mfma_f32_16x16x32_bf16 v[206:209], a[224:227], v[232:235], v[206:209]\n"
    "s_waitcnt lgkmcnt(0)\n"
    "v_cvt_pk_bf16_f32 v126, v126, v127\n"
    "v_cvt_pk_bf16_f32 v194, v194, v195\n"
    "v_mfma_f32_16x16x32_bf16 v[206:209], a[228:231], v[214:217], v[206:209]\n"
    "v_exp_f32 v196, v196\n"
    "v_mfma_f32_16x16x32_bf16 v[206:209], a[232:235], v[228:231], v[206:209]\n"
    "v_exp_f32 v197, v197\n"
    "v_mfma_f32_16x16x32_bf16 v[206:209], a[236:239], v[198:201], v[206:209]\n"
    "v_exp_f32 v190, v190\n"
    "v_mfma_f32_16x16x32_bf16 v[186:189], a[240:243], v[224:227], 0\n"
    "s_mov_b64 s[56:57], s[58:59]\n"
    "v_exp_f32 v191, v191\n"
    "v_mfma_f32_16x16x32_bf16 v[186:189], a[244:247], v[218:221], v[186:189]\n"
    "ds_read_b128 v[210:213], v244 offset:4096\n"
    "v_cvt_pk_bf16_f32 v127, v128, v129\n"
    "v_mfma_f32_16x16x32_bf16 v[186:189], a[248:251], v[232:235], v[186:189]\n"
    "s_add_u32 s85, s85, 256\n"
    "v_exp_f32 v192, v192\n"
    "v_mfma_f32_16x16x32_bf16 v[186:189], a[252:255], v[214:217], v[186:189]\n"
    "s_mov_b64 s[58:59], s[60:61]\n"
    "v_exp_f32 v193, v193\n"
    "v_mfma_f32_16x16x32_bf16 v[186:189], v[64:67], v[228:231], v[186:189]\n"
    "s_mul_hi_u32 s28, s85, 91382283\n"
    "v_exp_f32 v236, v236\n"
    "v_mfma_f32_16x16x32_bf16 v[186:189], v[68:71], v[198:201], v[186:189]\n"
    "s_mov_b64 s[60:61], s[62:63]\n"
    "v_exp_f32 v237, v237\n"
    "v_mfma_f32_16x16x32_bf16 v[130:133], v[72:75], v[224:227], 0\n"
    "s_mul_i32 s29, s28, 47\n"
    "v_exp_f32 v238, v238\n"
    "v_mfma_f32_16x16x32_bf16 v[130:133], v[76:79], v[218:221], v[130:133]\n"
    "s_mov_b64 s[62:63], s[64:65]\n"
    "v_exp_f32 v239, v239\n"
    "v_mfma_f32_16x16x32_bf16 v[130:133], v[80:83], v[232:235], v[130:133]\n"
    "s_sub_u32 s29, s85, s29\n"
    "v_cvt_pk_bf16_f32 v195, v196, v197\n"
    "v_mul_f32 v205, s82, v205\n"
    "v_mfma_f32_16x16x32_bf16 v[130:133], v[84:87], v[214:217], v[130:133]\n"
    "s_mul_i32 s64, s28, 1179648\n"
    "v_cvt_pk_bf16_f32 v128, v190, v191\n"
    "v_cvt_pk_bf16_f32 v129, v192, v193\n"
    "v_mfma_f32_16x16x32_bf16 v[130:133], v[88:91], v[228:231], v[130:133]\n"
    "ds_read_b128 v[230:233], v254 offset:6144\n"
    "v_cvt_pk_bf16_f32 v196, v236, v237\n"
    "v_mfma_f32_16x16x32_bf16 v[130:133], v[92:95], v[198:201], v[130:133]\n"
    "ds_read_b128 v[234:237], v248 offset:6144\n"
    "v_cvt_pk_bf16_f32 v197, v238, v239\n"
    "v_mfma_f32_16x16x32_bf16 v[190:193], v[158:161], v[102:105], v[16:19]\n"
    "ds_read_b128 v[238:241], v254 offset:6272\n"
    "v_cvt_pk_bf16_f32 v118, v202, v203\n"
    "v_mfma_f32_16x16x32_bf16 v[190:193], v[134:137], v[106:109], v[190:193]\n"
    "ds_read_b128 v[226:229], v248 offset:6272\n"
    "v_cvt_pk_bf16_f32 v119, v204, v205\n"
    "v_mfma_f32_16x16x32_bf16 v[222:225], v[162:165], v[102:105], v[20:23]\n"
    "s_mul_i32 s30, s29, 3072\n"
    "ds_read_b128 v[214:217], v254 offset:6400\n"
    "v_mfma_f32_16x16x32_bf16 v[222:225], v[136:139], v[104:107], v[222:225]\n"
    "ds_read_b128 v[202:205], v248 offset:6400\n"
    "v_cvt_pk_bf16_f32 v206, v206, v207\n"
    "v_mfma_f32_16x16x32_bf16 v[96:99], v[178:181], v[102:105], v[24:27]\n"
    "s_waitcnt lgkmcnt(6)\n"
    "buffer_store_dwordx4 v[210:213], v249, s[40:43], s22 offen offset:1024 sc1\n"
    "v_mfma_f32_16x16x32_bf16 v[96:99], v[140:143], v[106:109], v[96:99]\n"
    "s_add_u32 s64, s64, s30\n"
    "v_exp_f32 v190, v190\n"
    "v_mfma_f32_16x16x32_bf16 v[210:213], v[182:185], v[102:105], v[28:31]\n"
    "s_add_u32 s64, s64, 591360\n"
    "v_exp_f32 v191, v191\n"
    "v_mfma_f32_16x16x32_bf16 v[210:213], v[142:145], v[104:107], v[210:213]\n"
    "s_waitcnt lgkmcnt(3)\n"
    "v_exp_f32 v96, v96\n"
    "v_mfma_f32_16x16x32_bf16 v[198:201], a[216:219], v[230:233], 0\n"
    "v_exp_f32 v97, v97\n"
    "v_mfma_f32_16x16x32_bf16 v[198:201], a[220:223], v[234:237], v[198:201]\n"
    "v_exp_f32 v192, v192\n"
    "v_mfma_f32_16x16x32_bf16 v[198:201], a[224:227], v[238:241], v[198:201]\n"
    "s_waitcnt lgkmcnt(0)\n"
    "v_exp_f32 v193, v193\n"
    "v_mfma_f32_16x16x32_bf16 v[198:201], a[228:231], v[226:229], v[198:201]\n"
    "v_exp_f32 v98, v98\n"
    "v_mfma_f32_16x16x32_bf16 v[198:201], a[232:235], v[214:217], v[198:201]\n"
    "v_exp_f32 v99, v99\n"
    "v_mfma_f32_16x16x32_bf16 v[198:201], a[236:239], v[202:205], v[198:201]\n"
    "s_mul_i32 s65, s28, 48\n"
    "v_cvt_pk_bf16_f32 v190, v190, v191\n"
    "v_cvt_pk_bf16_f32 v96, v96, v97\n"
    "v_mfma_f32_16x16x32_bf16 v[104:107], a[240:243], v[230:233], 0\n"
    "s_add_u32 s65, s65, s29\n"
    "v_exp_f32 v222, v222\n"
    "v_mfma_f32_16x16x32_bf16 v[104:107], a[244:247], v[234:237], v[104:107]\n"
    "ds_read_b128 v[100:103], v244 offset:5120\n"
    "v_cvt_pk_bf16_f32 v191, v192, v193\n"
    "v_mfma_f32_16x16x32_bf16 v[104:107], a[248:251], v[238:241], v[104:107]\n"
    "s_lshl_b32 s65, s65, 13\n"
    "v_exp_f32 v223, v223\n"
    "v_mfma_f32_16x16x32_bf16 v[104:107], a[252:255], v[226:229], v[104:107]\n"
    "s_add_u32 s65, s65, s88\n"
    "v_exp_f32 v224, v224\n"
    "v_mfma_f32_16x16x32_bf16 v[104:107], v[64:67], v[214:217], v[104:107]\n"
    "s_xor_b32 s92, s92, 8192\n"
    "v_exp_f32 v225, v225\n"
    "v_mfma_f32_16x16x32_bf16 v[104:107], v[68:71], v[202:205], v[104:107]\n"
    "v_exp_f32 v210, v210\n"
    "v_mfma_f32_16x16x32_bf16 v[218:221], v[72:75], v[230:233], 0\n"
    "v_exp_f32 v211, v211\n"
    "v_mfma_f32_16x16x32_bf16 v[218:221], v[76:79], v[234:237], v[218:221]\n"
    "v_cvt_pk_bf16_f32 v97, v98, v99\n"
    "v_cvt_pk_bf16_f32 v207, v208, v209\n"
    "v_mfma_f32_16x16x32_bf16 v[218:221], v[80:83], v[238:241], v[218:221]\n"
    "v_exp_f32 v212, v212\n"
    "v_mfma_f32_16x16x32_bf16 v[218:221], v[84:87], v[226:229], v[218:221]\n"
    "v_exp_f32 v213, v213\n"
    "v_mfma_f32_16x16x32_bf16 v[218:221], v[88:91], v[214:217], v[218:221]\n"
    "v_cvt_pk_bf16_f32 v192, v222, v223\n"
    "v_cvt_pk_bf16_f32 v208, v186, v187\n"
    "v_mfma_f32_16x16x32_bf16 v[218:221], v[92:95], v[202:205], v[218:221]\n"
    "s_waitcnt lgkmcnt(0)\n"
    "buffer_store_dwordx4 v[100:103], v247, s[40:43], s22 offen offset:2048 sc1\n"
    "v_mfma_f32_16x16x32_bf16 v[226:229], v[120:123], v[126:129], 0\n"
    "v_cvt_pk_bf16_f32 v193, v224, v225\n"
    "v_cvt_pk_bf16_f32 v98, v210, v211\n"
    "v_mfma_f32_16x16x32_bf16 v[226:229], v[120:123], v[194:197], v[226:229]\n"
    "v_cvt_pk_bf16_f32 v99, v212, v213\n"
    "v_cvt_pk_bf16_f32 v209, v188, v189\n"
    "v_mfma_f32_16x16x32_bf16 v[202:205], v[150:153], v[126:129], 0\n"
    "ds_write_b128 v245, v[206:209] offset:0\n"
    "v_mfma_f32_16x16x32_bf16 v[202:205], v[170:173], v[194:197], v[202:205]\n"
    "v_rcp_f32 v227, v226\n"
    "v_mfma_f32_16x16x32_bf16 v[100:103], v[146:149], v[126:129], 0\n"
    "v_cvt_pk_bf16_f32 v130, v130, v131\n"
    "v_cvt_pk_bf16_f32 v131, v132, v133\n"
    "v_mfma_f32_16x16x32_bf16 v[100:103], v[166:169], v[194:197], v[100:103]\n"
    "ds_write_b64 v253, v[130:131] offset:0\n"
    "v_mfma_f32_16x16x32_bf16 v[186:189], v[154:157], v[126:129], 0\n"
    "v_mul_f32 v202, v227, v202\n"
    "v_mul_f32 v203, v227, v203\n"
    "v_mfma_f32_16x16x32_bf16 v[186:189], v[174:177], v[194:197], v[186:189]\n"
    "v_cvt_pk_bf16_f32 v202, v202, v203\n"
    "v_mul_f32 v204, v227, v204\n"
    "v_mfma_f32_16x16x32_bf16 v[128:131], v[158:161], v[108:111], v[32:35]\n"
    "v_mul_f32 v205, v227, v205\n"
    "v_cvt_pk_bf16_f32 v198, v198, v199\n"
    "v_mfma_f32_16x16x32_bf16 v[128:131], v[134:137], v[112:115], v[128:131]\n"
    "v_mul_f32 v186, v227, v186\n"
    "v_mul_f32 v187, v227, v187\n"
    "v_mfma_f32_16x16x32_bf16 v[124:127], v[162:165], v[108:111], v[36:39]\n"
    "v_cvt_pk_bf16_f32 v199, v200, v201\n"
    "v_cvt_pk_bf16_f32 v200, v104, v105\n"
    "v_mfma_f32_16x16x32_bf16 v[124:127], v[136:139], v[110:113], v[124:127]\n"
    "v_exp_f32 v128, v128\n"
    "v_mfma_f32_16x16x32_bf16 v[210:213], v[178:181], v[108:111], v[40:43]\n"
    "v_exp_f32 v129, v129\n"
    "v_mfma_f32_16x16x32_bf16 v[210:213], v[140:143], v[112:115], v[210:213]\n"
    "v_exp_f32 v130, v130\n"
    "v_mfma_f32_16x16x32_bf16 v[206:209], v[182:185], v[108:111], v[44:47]\n"
    "ds_read_b128 v[228:231], v254 offset:12288\n"
    "v_cvt_pk_bf16_f32 v128, v128, v129\n"
    "v_mfma_f32_16x16x32_bf16 v[206:209], v[142:145], v[110:113], v[206:209]\n"
    "ds_read_b128 v[232:235], v248 offset:12288\n"
    "v_cvt_pk_bf16_f32 v201, v106, v107\n"
    "v_mfma_f32_16x16x32_bf16 v[104:107], v[158:161], v[114:117], v[48:51]\n"
    "ds_read_b128 v[240:243], v254 offset:12416\n"
    "v_cvt_pk_bf16_f32 v218, v218, v219\n"
    "v_mfma_f32_16x16x32_bf16 v[104:107], v[134:137], v[118:121], v[104:107]\n"
    "ds_read_b128 v[236:239], v248 offset:12416\n"
    "v_cvt_pk_bf16_f32 v219, v220, v221\n"
    "v_mfma_f32_16x16x32_bf16 v[132:135], v[162:165], v[114:117], v[52:55]\n"
    "ds_read_b128 v[108:111], v254 offset:12544\n"
    "v_cvt_pk_bf16_f32 v203, v204, v205\n"
    "v_mfma_f32_16x16x32_bf16 v[132:135], v[136:139], v[116:119], v[132:135]\n"
    "ds_read_b128 v[162:165], v248 offset:12544\n"
    "v_mul_f32 v100, v227, v100\n"
    "v_mfma_f32_16x16x32_bf16 v[214:217], v[178:181], v[114:117], v[56:59]\n"
    "ds_write_b128 v245, v[198:201] offset:6144\n"
    "v_mfma_f32_16x16x32_bf16 v[214:217], v[140:143], v[118:121], v[214:217]\n"
    "ds_write_b64 v253, v[218:219] offset:6144\n"
    "v_mfma_f32_16x16x32_bf16 v[158:161], v[182:185], v[114:117], v[60:63]\n"
    "v_exp_f32 v210, v210\n"
    "v_mfma_f32_16x16x32_bf16 v[158:161], v[142:145], v[116:119], v[158:161]\n"
    "s_waitcnt lgkmcnt(5)\n"
    "v_exp_f32 v211, v211\n"
    "v_mfma_f32_16x16x32_bf16 v[222:225], a[216:219], v[228:231], 0\n"
    "v_exp_f32 v104, v104\n"
    "v_mfma_f32_16x16x32_bf16 v[222:225], a[220:223], v[232:235], v[222:225]\n"
    "v_exp_f32 v105, v105\n"
    "v_mfma_f32_16x16x32_bf16 v[222:225], a[224:227], v[240:243], v[222:225]\n"
    "s_waitcnt lgkmcnt(2)\n"
    "v_exp_f32 v214, v214\n"
    "v_mfma_f32_16x16x32_bf16 v[222:225], a[228:231], v[236:239], v[222:225]\n"
    "v_exp_f32 v215, v215\n"
    "v_mfma_f32_16x16x32_bf16 v[222:225], a[232:235], v[108:111], v[222:225]\n"
    "v_exp_f32 v131, v131\n"
    "v_mfma_f32_16x16x32_bf16 v[222:225], a[236:239], v[162:165], v[222:225]\n"
    "s_cbranch_vccnz .Lswin_P2_%=\n"
    ".Lswin_P2R_%=:\n"
    "v_exp_f32 v212, v212\n"
    "v_mfma_f32_16x16x32_bf16 v[194:197], a[240:243], v[228:231], 0\n"
    "v_exp_f32 v213, v213\n"
    "v_mfma_f32_16x16x32_bf16 v[194:197], a[244:247], v[232:235], v[194:197]\n"
    "v_cvt_pk_bf16_f32 v210, v210, v211\n"
    "v_cvt_pk_bf16_f32 v104, v104, v105\n"
    "v_mfma_f32_16x16x32_bf16 v[194:197], a[248:251], v[240:243], v[194:197]\n"
    "v_exp_f32 v106, v106\n"
    "v_mfma_f32_16x16x32_bf16 v[194:197], a[252:255], v[236:239], v[194:197]\n"
    "v_exp_f32 v107, v107\n"
    "v_mfma_f32_16x16x32_bf16 v[194:197], v[64:67], v[108:111], v[194:197]\n"
    "v_exp_f32 v216, v216\n"
    "v_mfma_f32_16x16x32_bf16 v[194:197], v[68:71], v[162:165], v[194:197]\n"
    "v_exp_f32 v217, v217\n"
    "v_mfma_f32_16x16x32_bf16 v[198:201], v[72:75], v[228:231], 0\n"
    "v_cvt_pk_bf16_f32 v214, v214, v215\n"
    "v_cvt_pk_bf16_f32 v129, v130, v131\n"
    "v_mfma_f32_16x16x32_bf16 v[198:201], v[76:79], v[232:235], v[198:201]\n"
    "v_exp_f32 v124, v124\n"
    "v_mfma_f32_16x16x32_bf16 v[198:201], v[80:83], v[240:243], v[198:201]\n"
    "v_exp_f32 v125, v125\n"
    "v_mfma_f32_16x16x32_bf16 v[198:201], v[84:87], v[236:239], v[198:201]\n"
    "v_exp_f32 v126, v126\n"
    "v_mfma_f32_16x16x32_bf16 v[198:201], v[88:91], v[108:111], v[198:201]\n"
    "ds_read_b128 v[236:239], v254 offset:18432\n"
    "v_cvt_pk_bf16_f32 v211, v212, v213\n"
    "v_mfma_f32_16x16x32_bf16 v[198:201], v[92:95], v[162:165], v[198:201]\n"
    "ds_read_b128 v[240:243], v248 offset:18432\n"
    "v_cvt_pk_bf16_f32 v222, v222, v223\n"
    "v_mfma_f32_16x16x32_bf16 v[228:231], v[120:123], v[190:193], 0\n"
    "ds_read_b128 v[112:115], v254 offset:18560\n"
    "v_mul_f32 v101, v227, v101\n"
    "v_mfma_f32_16x16x32_bf16 v[228:231], v[120:123], v[96:99], v[228:231]\n"
    "ds_read_b128 v[116:119], v248 offset:18560\n"
    "v_mul_f32 v102, v227, v102\n"
    "v_mfma_f32_16x16x32_bf16 v[232:235], v[150:153], v[190:193], 0\n"
    "ds_read_b128 v[138:141], v254 offset:18688\n"
    "v_mul_f32 v103, v227, v103\n"
    "v_mfma_f32_16x16x32_bf16 v[232:235], v[170:173], v[96:99], v[232:235]\n"
    "ds_read_b128 v[182:185], v248 offset:18688\n"
    "v_cvt_pk_bf16_f32 v186, v186, v187\n"
    "v_mfma_f32_16x16x32_bf16 v[178:181], v[146:149], v[190:193], 0\n"
    "s_cmp_eq_u32 s84, 5\n"
    "s_cselect_b64 s[64:65], s[68:69], s[64:65]\n"
    "v_mul_f32 v188, v227, v188\n"
    "v_mfma_f32_16x16x32_bf16 v[178:181], v[166:169], v[96:99], v[178:181]\n"
    "v_rcp_f32 v229, v228\n"
    "v_mfma_f32_16x16x32_bf16 v[218:221], v[154:157], v[190:193], 0\n"
    "v_mul_f32 v232, v229, v232\n"
    "v_mul_f32 v233, v229, v233\n"
    "v_mfma_f32_16x16x32_bf16 v[218:221], v[174:177], v[96:99], v[218:221]\n"
    "s_waitcnt lgkmcnt(3)\n"
    "v_cvt_pk_bf16_f32 v232, v232, v233\n"
    "v_mul_f32 v234, v229, v234\n"
    "v_mfma_f32_16x16x32_bf16 v[108:111], a[216:219], v[236:239], 0\n"
    "v_mul_f32 v235, v229, v235\n"
    "v_mul_f32 v189, v227, v189\n"
    "v_mfma_f32_16x16x32_bf16 v[108:111], a[220:223], v[240:243], v[108:111]\n"
    "v_mul_f32 v218, v229, v218\n"
    "v_mul_f32 v219, v229, v219\n"
    "v_mfma_f32_16x16x32_bf16 v[108:111], a[224:227], v[112:115], v[108:111]\n"
    "s_waitcnt lgkmcnt(0)\n"
    "v_exp_f32 v127, v127\n"
    "v_mfma_f32_16x16x32_bf16 v[108:111], a[228:231], v[116:119], v[108:111]\n"
    "v_exp_f32 v206, v206\n"
    "v_mfma_f32_16x16x32_bf16 v[108:111], a[232:235], v[138:141], v[108:111]\n"
    "v_exp_f32 v207, v207\n"
    "v_mfma_f32_16x16x32_bf16 v[108:111], a[236:239], v[182:185], v[108:111]\n"
    "v_exp_f32 v208, v208\n"
    "v_mfma_f32_16x16x32_bf16 v[162:165], a[240:243], v[236:239], 0\n"
    "v_exp_f32 v209, v209\n"
    "v_mfma_f32_16x16x32_bf16 v[162:165], a[244:247], v[240:243], v[162:165]\n"
    "v_cvt_pk_bf16_f32 v233, v234, v235\n"
    "v_mul_f32 v178, v229, v178\n"
    "v_mfma_f32_16x16x32_bf16 v[162:165], a[248:251], v[112:115], v[162:165]\n"
    "v_mul_f32 v179, v229, v179\n"
    "v_mul_f32 v180, v229, v180\n"
    "v_mfma_f32_16x16x32_bf16 v[162:165], a[252:255], v[116:119], v[162:165]\n"
    "v_exp_f32 v132, v132\n"
    "v_mfma_f32_16x16x32_bf16 v[162:165], v[64:67], v[138:141], v[162:165]\n"
    "s_cmp_gt_u32 s84, 5\n"
    "s_cselect_b32 s64, 0x80000000, s64\n"
    "s_cselect_b32 s65, 0x80000000, s65\n"
    "v_mfma_f32_16x16x32_bf16 v[162:165], v[68:71], v[182:185], v[162:165]\n"
    "s_add_u32 s84, s84, 1\n"
    "v_exp_f32 v133, v133\n"
    "v_mfma_f32_16x16x32_bf16 v[96:99], v[72:75], v[236:239], 0\n"
    "v_exp_f32 v134, v134\n"
    "v_mfma_f32_16x16x32_bf16 v[96:99], v[76:79], v[240:243], v[96:99]\n"
    "v_exp_f32 v135, v135\n"
    "v_mfma_f32_16x16x32_bf16 v[96:99], v[80:83], v[112:115], v[96:99]\n"
    "v_mul_f32 v181, v229, v181\n"
    "v_cvt_pk_bf16_f32 v105, v106, v107\n"
    "v_mfma_f32_16x16x32_bf16 v[96:99], v[84:87], v[116:119], v[96:99]\n"
    "v_exp_f32 v158, v158\n"
    "v_mfma_f32_16x16x32_bf16 v[96:99], v[88:91], v[138:141], v[96:99]\n"
    "v_exp_f32 v159, v159\n"
    "v_mfma_f32_16x16x32_bf16 v[96:99], v[92:95], v[182:185], v[96:99]\n"
    "v_add_u32 v254, s95, v254\n"
    "v_cvt_pk_bf16_f32 v218, v218, v219\n"
    "v_cvt_pk_bf16_f32 v130, v124, v125\n"
    "v_cvt_pk_bf16_f32 v131, v126, v127\n"
    "s_nop 1\n"
    "v_mfma_f32_16x16x32_bf16 v[236:239], v[120:123], v[128:131], 0\n"
    "v_mul_f32 v220, v229, v220\n"
    "v_mul_f32 v221, v229, v221\n"
    "v_cvt_pk_bf16_f32 v212, v206, v207\n"
    "v_cvt_pk_bf16_f32 v213, v208, v209\n"
    "s_nop 1\n"
    "v_mfma_f32_16x16x32_bf16 v[236:239], v[120:123], v[210:213], v[236:239]\n"
    "v_exp_f32 v160, v160\n"
    "v_mfma_f32_16x16x32_bf16 v[190:193], v[150:153], v[128:131], 0\n"
    "v_exp_f32 v161, v161\n"
    "v_mfma_f32_16x16x32_bf16 v[190:193], v[170:173], v[210:213], v[190:193]\n"
    "v_cvt_pk_bf16_f32 v215, v216, v217\n"
    "v_add_u32 v248, s95, v248\n"
    "v_mfma_f32_16x16x32_bf16 v[206:209], v[146:149], v[128:131], 0\n"
    "v_cvt_pk_bf16_f32 v223, v224, v225\n"
    "v_cvt_pk_bf16_f32 v204, v100, v101\n"
    "v_mfma_f32_16x16x32_bf16 v[206:209], v[166:169], v[210:213], v[206:209]\n"
    "v_cvt_pk_bf16_f32 v205, v102, v103\n"
    "v_cvt_pk_bf16_f32 v187, v188, v189\n"
    "v_mfma_f32_16x16x32_bf16 v[182:185], v[154:157], v[128:131], 0\n"
    "ds_read_b128 v[138:141], v254 offset:0\n"
    "v_cvt_pk_bf16_f32 v198, v198, v199\n"
    "v_mfma_f32_16x16x32_bf16 v[182:185], v[174:177], v[210:213], v[182:185]\n"
    "ds_read_b128 v[100:103], v248 offset:0\n"
    "v_max3_f32 v255, v255, v226, v227\n"
    "v_cvt_pk_bf16_f32 v106, v132, v133\n"
    "v_cvt_pk_bf16_f32 v107, v134, v135\n"
    "s_nop 1\n"
    "v_mfma_f32_16x16x32_bf16 v[132:135], v[120:123], v[104:107], 0\n"
    "ds_read_b128 v[124:127], v254 offset:128\n"
    "v_cvt_pk_bf16_f32 v234, v178, v179\n"
    "v_cvt_pk_bf16_f32 v216, v158, v159\n"
    "v_cvt_pk_bf16_f32 v217, v160, v161\n"
    "s_nop 1\n"
    "v_mfma_f32_16x16x32_bf16 v[132:135], v[120:123], v[214:217], v[132:135]\n"
    "ds_read_b128 v[112:115], v248 offset:128\n"
    "v_cvt_pk_bf16_f32 v235, v180, v181\n"
    "v_mfma_f32_16x16x32_bf16 v[178:181], v[150:153], v[104:107], 0\n"
    "ds_read_b128 v[116:119], v254 offset:256\n"
    "v_cvt_pk_bf16_f32 v219, v220, v221\n"
    "v_mfma_f32_16x16x32_bf16 v[178:181], v[170:173], v[214:217], v[178:181]\n"
    "ds_read_b128 v[128:131], v248 offset:256\n"
    "v_cvt_pk_bf16_f32 v224, v194, v195\n"
    "v_mfma_f32_16x16x32_bf16 v[150:153], v[146:149], v[104:107], 0\n"
    "ds_write_b128 v246, v[202:205] offset:0\n"
    "v_mfma_f32_16x16x32_bf16 v[150:153], v[166:169], v[214:217], v[150:153]\n"
    "ds_write_b64 v252, v[186:187] offset:0\n"
    "v_mfma_f32_16x16x32_bf16 v[144:147], v[154:157], v[104:107], 0\n"
    "ds_write_b128 v246, v[232:235] offset:6144\n"
    "v_mfma_f32_16x16x32_bf16 v[144:147], v[174:177], v[214:217], v[144:147]\n"
    "ds_write_b64 v252, v[218:219] offset:6144\n"
    "v_cvt_pk_bf16_f32 v225, v196, v197\n"
    "ds_write_b128 v245, v[222:225] offset:12288\n"
    "v_cvt_pk_bf16_f32 v199, v200, v201\n"
    "ds_write_b64 v253, v[198:199] offset:12288\n"
    "v_max3_f32 v255, v255, v228, v229\n"
    "s_cmp_lg_u32 s84, 9\n"
    "s_cbranch_scc1 .Lswin_LOOP_%=\n"
    "v_rcp_f32 v237, v236\n"
    "s_nop 0\n"
    "v_max3_f32 v255, v255, v236, v237\n"
    "v_mul_f32 v190, v237, v190\n"
    "v_mul_f32 v191, v237, v191\n"
    "v_cvt_pk_bf16_f32 v190, v190, v191\n"
    "v_mul_f32 v192, v237, v192\n"
    "v_mul_f32 v193, v237, v193\n"
    "v_cvt_pk_bf16_f32 v191, v192, v193\n"
    "v_mul_f32 v206, v237, v206\n"
    "v_mul_f32 v207, v237, v207\n"
    "v_cvt_pk_bf16_f32 v192, v206, v207\n"
    "v_mul_f32 v208, v237, v208\n"
    "v_mul_f32 v209, v237, v209\n"
    "v_cvt_pk_bf16_f32 v193, v208, v209\n"
    "v_mul_f32 v182, v237, v182\n"
    "v_mul_f32 v183, v237, v183\n"
    "v_cvt_pk_bf16_f32 v182, v182, v183\n"
    "v_mul_f32 v184, v237, v184\n"
    "v_mul_f32 v185, v237, v185\n"
    "v_cvt_pk_bf16_f32 v183, v184, v185\n"
    "v_cvt_pk_bf16_f32 v108, v108, v109\n"
    "v_rcp_f32 v133, v132\n"
    "s_nop 0\n"
    "v_max3_f32 v255, v255, v132, v133\n"
    "v_mul_f32 v178, v133, v178\n"
    "v_mul_f32 v179, v133, v179\n"
    "v_cvt_pk_bf16_f32 v178, v178, v179\n"
    "v_mul_f32 v180, v133, v180\n"
    "v_mul_f32 v181, v133, v181\n"
    "v_cvt_pk_bf16_f32 v179, v180, v181\n"
    "v_mul_f32 v150, v133, v150\n"
    "v_mul_f32 v151, v133, v151\n"
    "v_cvt_pk_bf16_f32 v180, v150, v151\n"
    "v_mul_f32 v152, v133, v152\n"
    "v_mul_f32 v153, v133, v153\n"
    "v_cvt_pk_bf16_f32 v181, v152, v153\n"
    "v_mul_f32 v144, v133, v144\n"
    "v_mul_f32 v145, v133, v145\n"
    "v_cvt_pk_bf16_f32 v144, v144, v145\n"
    "v_mul_f32 v146, v133, v146\n"
    "ds_write_b128 v246, v[190:193] offset:12288\n"
    "v_mul_f32 v147, v133, v147\n"
    "v_cvt_pk_bf16_f32 v145, v146, v147\n"
    "v_cvt_pk_bf16_f32 v109, v110, v111\n"
    "v_cvt_pk_bf16_f32 v110, v162, v163\n"
    "v_cvt_pk_bf16_f32 v111, v164, v165\n"
    "v_cvt_pk_bf16_f32 v96, v96, v97\n"
    "v_cvt_pk_bf16_f32 v97, v98, v99\n"
    "ds_write_b64 v252, v[182:183] offset:12288\n"
    "ds_write_b128 v246, v[178:181] offset:18432\n"
    "ds_write_b64 v252, v[144:145] offset:18432\n"
    "ds_write_b128 v245, v[108:111] offset:18432\n"
    "ds_write_b64 v253, v[96:97] offset:18432\n"
    "s_mov_b32 s93, 0x53800000\n"
    "v_cmp_lt_f32_e64 s[76:77], s93, v255\n"
    "s_sub_u32 s23, s91, s89\n"
    "v_add_u32 v254, s23, v254\n"
    "v_add_u32 v248, s23, v248\n"
    "s_add_u32 s94, s88, 155648\n"
    "s_or_b32 s93, s76, s77\n"
    "v_mov_b32 v0, s93\n"
    "v_mov_b32 v1, s94\n"
    "s_waitcnt vmcnt(12)\n"
    "ds_write_b32 v1, v0\n"
    "s_add_u32 s94, s90, s87\n"
    "v_add_u32 v2, s94, v250\n"
    "v_mov_b32 v1, 155648\n"
    "s_waitcnt lgkmcnt(0)\n"
    "s_barrier\n"
    "ds_read_b128 v[96:99], v254 offset:0\n"
    "ds_read_b128 v[100:103], v248 offset:0\n"
    "ds_read_b128 v[104:107], v254 offset:128\n"
    "ds_read_b128 v[108:111], v248 offset:128\n"
    "ds_read_b128 v[112:115], v254 offset:256\n"
    "ds_read_b128 v[116:119], v248 offset:256\n"
    "ds_read_b128 v[124:127], v254 offset:6144\n"
    "ds_read_b128 v[128:131], v248 offset:6144\n"
    "ds_read_b128 v[132:135], v254 offset:6272\n"
    "ds_read_b128 v[138:141], v248 offset:6272\n"
    "ds_read_b128 v[144:147], v254 offset:6400\n"
    "ds_read_b128 v[148:151], v248 offset:6400\n"
    "ds_read2st64_b32 v[4:5], v1 offset0:0 offset1:8\n"
    "ds_read2st64_b32 v[6:7], v1 offset0:16 offset1:24\n"
    "ds_read_b128 v[12:15], v2 offset:0\n"
    "ds_read_b128 v[16:19], v2 offset:1024\n"
    "ds_read_b128 v[20:23], v2 offset:2048\n"
    "ds_read_b128 v[24:27], v2 offset:3072\n"
    "ds_read_b128 v[28:31], v2 offset:4096\n"
    "ds_read_b128 v[32:35], v2 offset:5120\n"
    "ds_read_b128 v[152:155], v254 offset:12288\n"
    "ds_read_b128 v[156:159], v248 offset:12288\n"
    "ds_read_b128 v[160:163], v254 offset:12416\n"
    "ds_read_b128 v[164:167], v248 offset:12416\n"
    "ds_read_b128 v[168:171], v254 offset:12544\n"
    "ds_read_b128 v[172:175], v248 offset:12544\n"
    "ds_read_b128 v[176:179], v254 offset:18432\n"
    "ds_read_b128 v[180:183], v248 offset:18432\n"
    "ds_read_b128 v[184:187], v254 offset:18560\n"
    "ds_read_b128 v[188:191], v248 offset:18560\n"
    "ds_read_b128 v[192:195], v254 offset:18688\n"
    "ds_read_b128 v[196:199], v248 offset:18688\n"
    "s_sub_u32 s24, s89, s90\n"
    "v_add_u32 v245, s24, v245\n"
    "v_add_u32 v253, s24, v253\n"
    "s_waitcnt lgkmcnt(15)\n"
    "v_mfma_f32_16x16x32_bf16 v[200:203], a[216:219], v[96:99], 0\n"
    "s_add_u32 s22, s56, s86\n"
    "buffer_store_dwordx4 v[12:15], v251, s[40:43], s22 offen offset:0 sc1\n"
    "v_mfma_f32_16x16x32_bf16 v[200:203], a[220:223], v[100:103], v[200:203]\n"
    "v_mfma_f32_16x16x32_bf16 v[200:203], a[224:227], v[104:107], v[200:203]\n"
    "v_mfma_f32_16x16x32_bf16 v[200:203], a[228:231], v[108:111], v[200:203]\n"
    "buffer_store_dwordx4 v[16:19], v249, s[40:43], s22 offen offset:1024 sc1\n"
    "v_mfma_f32_16x16x32_bf16 v[200:203], a[232:235], v[112:115], v[200:203]\n"
    "v_mfma_f32_16x16x32_bf16 v[200:203], a[236:239], v[116:119], v[200:203]\n"
    "v_mfma_f32_16x16x32_bf16 v[204:207], a[240:243], v[96:99], 0\n"
    "buffer_store_dwordx4 v[20:23], v247, s[40:43], s22 offen offset:2048 sc1\n"
    "v_mfma_f32_16x16x32_bf16 v[204:207], a[244:247], v[100:103], v[204:207]\n"
    "v_mfma_f32_16x16x32_bf16 v[204:207], a[248:251], v[104:107], v[204:207]\n"
    "s_waitcnt lgkmcnt(0)\n"
    "v_mfma_f32_16x16x32_bf16 v[204:207], a[252:255], v[108:111], v[204:207]\n"
    "s_add_u32 s22, s22, 147456\n"
    "buffer_store_dwordx4 v[24:27], v251, s[40:43], s22 offen offset:0 sc1\n"
    "v_mfma_f32_16x16x32_bf16 v[204:207], v[64:67], v[112:115], v[204:207]\n"
    "v_mfma_f32_16x16x32_bf16 v[204:207], v[68:71], v[116:119], v[204:207]\n"
    "v_mfma_f32_16x16x32_bf16 v[208:211], v[72:75], v[96:99], 0\n"
    "buffer_store_dwordx4 v[28:31], v249, s[40:43], s22 offen offset:1024 sc1\n"
    "v_mfma_f32_16x16x32_bf16 v[208:211], v[76:79], v[100:103], v[208:211]\n"
    "v_mfma_f32_16x16x32_bf16 v[208:211], v[80:83], v[104:107], v[208:211]\n"
    "v_cvt_pk_bf16_f32 v200, v200, v201\n"
    "v_cvt_pk_bf16_f32 v201, v202, v203\n"
    "v_mfma_f32_16x16x32_bf16 v[208:211], v[84:87], v[108:111], v[208:211]\n"
    "v_cvt_pk_bf16_f32 v202, v204, v205\n"
    "v_cvt_pk_bf16_f32 v203, v206, v207\n"
    "v_mfma_f32_16x16x32_bf16 v[208:211], v[88:91], v[112:115], v[208:211]\n"
    "ds_write_b128 v245, v[200:203] offset:0\n"
    "v_mfma_f32_16x16x32_bf16 v[208:211], v[92:95], v[116:119], v[208:211]\n"
    "v_mfma_f32_16x16x32_bf16 v[212:215], a[216:219], v[124:127], 0\n"
    "buffer_store_dwordx4 v[32:35], v247, s[40:43], s22 offen offset:2048 sc1\n"
    "v_mfma_f32_16x16x32_bf16 v[212:215], a[220:223], v[128:131], v[212:215]\n"
    "v_mfma_f32_16x16x32_bf16 v[212:215], a[224:227], v[132:135], v[212:215]\n"
    "v_cvt_pk_bf16_f32 v208, v208, v209\n"
    "v_cvt_pk_bf16_f32 v209, v210, v211\n"
    "v_mfma_f32_16x16x32_bf16 v[212:215], a[228:231], v[138:141], v[212:215]\n"
    "ds_write_b64 v253, v[208:209] offset:0\n"
    "v_mfma_f32_16x16x32_bf16 v[212:215], a[232:235], v[144:147], v[212:215]\n"
    "v_mfma_f32_16x16x32_bf16 v[212:215], a[236:239], v[148:151], v[212:215]\n"
    "v_mfma_f32_16x16x32_bf16 v[216:219], a[240:243], v[124:127], 0\n"
    "v_or3_b32 v4, v4, v5, v6\n"
    "v_mfma_f32_16x16x32_bf16 v[216:219], a[244:247], v[128:131], v[216:219]\n"
    "v_mfma_f32_16x16x32_bf16 v[216:219], a[248:251], v[132:135], v[216:219]\n"
    "v_mfma_f32_16x16x32_bf16 v[216:219], a[252:255], v[138:141], v[216:219]\n"
    "v_or_b32 v4, v4, v7\n"
    "v_mfma_f32_16x16x32_bf16 v[216:219], v[64:67], v[144:147], v[216:219]\n"
    "v_mfma_f32_16x16x32_bf16 v[216:219], v[68:71], v[148:151], v[216:219]\n"
    "v_mfma_f32_16x16x32_bf16 v[220:223], v[72:75], v[124:127], 0\n"
    "s_nop 0\n"
    "v_readfirstlane_b32 s27, v4\n"
    "v_mfma_f32_16x16x32_bf16 v[220:223], v[76:79], v[128:131], v[220:223]\n"
    "v_mfma_f32_16x16x32_bf16 v[220:223], v[80:83], v[132:135], v[220:223]\n"
    "v_cvt_pk_bf16_f32 v212, v212, v213\n"
    "v_cvt_pk_bf16_f32 v213, v214, v215\n"
    "v_mfma_f32_16x16x32_bf16 v[220:223], v[84:87], v[138:141], v[220:223]\n"
    "v_cvt_pk_bf16_f32 v214, v216, v217\n"
    "v_cvt_pk_bf16_f32 v215, v218, v219\n"
    "v_mfma_f32_16x16x32_bf16 v[220:223], v[88:91], v[144:147], v[220:223]\n"
    "ds_write_b128 v245, v[212:215] offset:6144\n"
    "v_mfma_f32_16x16x32_bf16 v[220:223], v[92:95], v[148:151], v[220:223]\n"
    "v_mfma_f32_16x16x32_bf16 v[224:227], a[216:219], v[152:155], 0\n"
    "v_mfma_f32_16x16x32_bf16 v[224:227], a[220:223], v[156:159], v[224:227]\n"
    "v_mfma_f32_16x16x32_bf16 v[224:227], a[224:227], v[160:163], v[224:227]\n"
    "v_cvt_pk_bf16_f32 v220, v220, v221\n"
    "v_cvt_pk_bf16_f32 v221, v222, v223\n"
    "v_mfma_f32_16x16x32_bf16 v[224:227], a[228:231], v[164:167], v[224:227]\n"
    "ds_write_b64 v253, v[220:221] offset:6144\n"
    "v_mfma_f32_16x16x32_bf16 v[224:227], a[232:235], v[168:171], v[224:227]\n"
    "v_mfma_f32_16x16x32_bf16 v[224:227], a[236:239], v[172:175], v[224:227]\n"
    "v_mfma_f32_16x16x32_bf16 v[228:231], a[240:243], v[152:155], 0\n"
    "v_mfma_f32_16x16x32_bf16 v[228:231], a[244:247], v[156:159], v[228:231]\n"
    "v_mfma_f32_16x16x32_bf16 v[228:231], a[248:251], v[160:163], v[228:231]\n"
    "v_mfma_f32_16x16x32_bf16 v[228:231], a[252:255], v[164:167], v[228:231]\n"
    "v_mfma_f32_16x16x32_bf16 v[228:231], v[64:67], v[168:171], v[228:231]\n"
    "v_mfma_f32_16x16x32_bf16 v[228:231], v[68:71], v[172:175], v[228:231]\n"
    "v_mfma_f32_16x16x32_bf16 v[232:235], v[72:75], v[152:155], 0\n"
    "v_mfma_f32_16x16x32_bf16 v[232:235], v[76:79], v[156:159], v[232:235]\n"
    "v_mfma_f32_16x16x32_bf16 v[232:235], v[80:83], v[160:163], v[232:235]\n"
    "v_cvt_pk_bf16_f32 v224, v224, v225\n"
    "v_cvt_pk_bf16_f32 v225, v226, v227\n"
    "v_mfma_f32_16x16x32_bf16 v[232:235], v[84:87], v[164:167], v[232:235]\n"
    "v_cvt_pk_bf16_f32 v226, v228, v229\n"
    "v_cvt_pk_bf16_f32 v227, v230, v231\n"
    "v_mfma_f32_16x16x32_bf16 v[232:235], v[88:91], v[168:171], v[232:235]\n"
    "ds_write_b128 v245, v[224:227] offset:12288\n"
    "v_mfma_f32_16x16x32_bf16 v[232:235], v[92:95], v[172:175], v[232:235]\n"
    "v_mfma_f32_16x16x32_bf16 v[236:239], a[216:219], v[176:179], 0\n"
    "v_mfma_f32_16x16x32_bf16 v[236:239], a[220:223], v[180:183], v[236:239]\n"
    "v_mfma_f32_16x16x32_bf16 v[236:239], a[224:227], v[184:187], v[236:239]\n"
    "v_cvt_pk_bf16_f32 v232, v232, v233\n"
    "v_cvt_pk_bf16_f32 v233, v234, v235\n"
    "v_mfma_f32_16x16x32_bf16 v[236:239], a[228:231], v[188:191], v[236:239]\n"
    "ds_write_b64 v253, v[232:233] offset:12288\n"
    "v_mfma_f32_16x16x32_bf16 v[236:239], a[232:235], v[192:195], v[236:239]\n"
    "v_mfma_f32_16x16x32_bf16 v[236:239], a[236:239], v[196:199], v[236:239]\n"
    "v_mfma_f32_16x16x32_bf16 v[240:243], a[240:243], v[176:179], 0\n"
    "v_mfma_f32_16x16x32_bf16 v[240:243], a[244:247], v[180:183], v[240:243]\n"
    "v_mfma_f32_16x16x32_bf16 v[240:243], a[248:251], v[184:187], v[240:243]\n"
    "v_mfma_f32_16x16x32_bf16 v[240:243], a[252:255], v[188:191], v[240:243]\n"
    "v_mfma_f32_16x16x32_bf16 v[240:243], v[64:67], v[192:195], v[240:243]\n"
    "v_mfma_f32_16x16x32_bf16 v[240:243], v[68:71], v[196:199], v[240:243]\n"
    "v_mfma_f32_16x16x32_bf16 v[8:11], v[72:75], v[176:179], 0\n"
    "v_mfma_f32_16x16x32_bf16 v[8:11], v[76:79], v[180:183], v[8:11]\n"
    "v_mfma_f32_16x16x32_bf16 v[8:11], v[80:83], v[184:187], v[8:11]\n"
    "v_cvt_pk_bf16_f32 v236, v236, v237\n"
    "v_cvt_pk_bf16_f32 v237, v238, v239\n"
    "v_mfma_f32_16x16x32_bf16 v[8:11], v[84:87], v[188:191], v[8:11]\n"
    "v_cvt_pk_bf16_f32 v238, v240, v241\n"
    "v_cvt_pk_bf16_f32 v239, v242, v243\n"
    "v_mfma_f32_16x16x32_bf16 v[8:11], v[88:91], v[192:195], v[8:11]\n"
    "ds_write_b128 v245, v[236:239] offset:18432\n"
    "v_mfma_f32_16x16x32_bf16 v[8:11], v[92:95], v[196:199], v[8:11]\n"
    "s_cmp_eq_u32 s25, 6\n"
    "s_cbranch_scc1 .Lswin_PS_%=\n"
    ".Lswin_PSR_%=:\n"
    "s_add_u32 s94, s89, s87\n"
    "v_add_u32 v2, s94, v250\n"
    "s_nop 4\n"
    "v_cvt_pk_bf16_f32 v8, v8, v9\n"
    "v_cvt_pk_bf16_f32 v9, v10, v11\n"
    "ds_write_b64 v253, v[8:9] offset:18432\n"
    "s_waitcnt lgkmcnt(0)\n"
    "s_barrier\n"
    "ds_read_b128 v[96:99], v2 offset:0\n"
    "ds_read_b128 v[100:103], v2 offset:1024\n"
    "ds_read_b128 v[104:107], v2 offset:2048\n"
    "ds_read_b128 v[108:111], v2 offset:3072\n"
    "ds_read_b128 v[112:115], v2 offset:4096\n"
    "ds_read_b128 v[116:119], v2 offset:5120\n"
    "s_add_u32 s22, s58, s86\n"
    "s_waitcnt lgkmcnt(5)\n"
    "buffer_store_dwordx4 v[96:99], v251, s[40:43], s22 offen offset:0 sc1\n"
    "s_waitcnt lgkmcnt(4)\n"
    "buffer_store_dwordx4 v[100:103], v249, s[40:43], s22 offen offset:1024 sc1\n"
    "s_waitcnt lgkmcnt(3)\n"
    "buffer_store_dwordx4 v[104:107], v247, s[40:43], s22 offen offset:2048 sc1\n"
    "s_add_u32 s22, s22, 147456\n"
    "s_waitcnt lgkmcnt(2)\n"
    "buffer_store_dwordx4 v[108:111], v251, s[40:43], s22 offen offset:0 sc1\n"
    "s_waitcnt lgkmcnt(1)\n"
    "buffer_store_dwordx4 v[112:115], v249, s[40:43], s22 offen offset:1024 sc1\n"
    "s_waitcnt lgkmcnt(0)\n"
    "buffer_store_dwordx4 v[116:119], v247, s[40:43], s22 offen offset:2048 sc1\n"
    "s_mov_b32 %0, s27\n"
    "s_cmp_lg_u32 s27, 0\n"
    "s_cbranch_scc1 .Lswin_END_%=\n"
    "s_endpgm\n"
    ".Lswin_CEND_%=:\n"
    "s_branch .Lswin_END_%=\n"
    ".Lswin_P1_%=:\n"
    "s_nop 15\n"
    "v_lshrrev_b32 v133, 8, v250\n"
    "v_bfe_u32 v132, v250, 4, 4\n"
    "v_lshlrev_b32 v132, 7, v132\n"
    "v_lshl_add_u32 v132, v133, 3, v132\n"
    "v_add_u32 v132, s92, v132\n"
    "ds_read_b64 v[124:125], v132 offset:0\n"
    "ds_read_b64 v[126:127], v132 offset:32\n"
    "ds_read_b64 v[128:129], v132 offset:64\n"
    "ds_read_b64 v[130:131], v132 offset:96\n"
    "s_waitcnt lgkmcnt(0)\n"
    "v_lshlrev_b32 v133, 16, v124\n"
    "v_fmac_f32 v0, s83, v133\n"
    "v_and_b32 v133, 0xffff0000, v124\n"
    "v_fmac_f32 v1, s83, v133\n"
    "v_lshlrev_b32 v133, 16, v125\n"
    "v_fmac_f32 v2, s83, v133\n"
    "v_and_b32 v133, 0xffff0000, v125\n"
    "v_fmac_f32 v3, s83, v133\n"
    "v_lshlrev_b32 v133, 16, v126\n"
    "v_fmac_f32 v4, s83, v133\n"
    "v_and_b32 v133, 0xffff0000, v126\n"
    "v_fmac_f32 v5, s83, v133\n"
    "v_lshlrev_b32 v133, 16, v127\n"
    "v_fmac_f32 v6, s83, v133\n"
    "v_and_b32 v133, 0xffff0000, v127\n"
    "v_fmac_f32 v7, s83, v133\n"
    "v_lshlrev_b32 v133, 16, v128\n"
    "v_fmac_f32 v8, s83, v133\n"
    "v_and_b32 v133, 0xffff0000, v128\n"
    "v_fmac_f32 v9, s83, v133\n"
    "v_lshlrev_b32 v133, 16, v129\n"
    "v_fmac_f32 v10, s83, v133\n"
    "v_and_b32 v133, 0xffff0000, v129\n"
    "v_fmac_f32 v11, s83, v133\n"
    "v_lshlrev_b32 v133, 16, v130\n"
    "v_fmac_f32 v12, s83, v133\n"
    "v_and_b32 v133, 0xffff0000, v130\n"
    "v_fmac_f32 v13, s83, v133\n"
    "v_lshlrev_b32 v133, 16, v131\n"
    "v_fmac_f32 v14, s83, v133\n"
    "v_and_b32 v133, 0xffff0000, v131\n"
    "v_fmac_f32 v15, s83, v133\n"
    "ds_read_b64 v[124:125], v132 offset:2048\n"
    "ds_read_b64 v[126:127], v132 offset:2080\n"
    "ds_read_b64 v[128:129], v132 offset:2112\n"
    "ds_read_b64 v[130:131], v132 offset:2144\n"
    "s_waitcnt lgkmcnt(0)\n"
    "v_lshlrev_b32 v133, 16, v124\n"
    "v_fmac_f32 v16, s83, v133\n"
    "v_and_b32 v133, 0xffff0000, v124\n"
    "v_fmac_f32 v17, s83, v133\n"
    "v_lshlrev_b32 v133, 16, v125\n"
    "v_fmac_f32 v18, s83, v133\n"
    "v_and_b32 v133, 0xffff0000, v125\n"
    "v_fmac_f32 v19, s83, v133\n"
    "v_lshlrev_b32 v133, 16, v126\n"
    "v_fmac_f32 v20, s83, v133\n"
    "v_and_b32 v133, 0xffff0000, v126\n"
    "v_fmac_f32 v21, s83, v133\n"
    "v_lshlrev_b32 v133, 16, v127\n"
    "v_fmac_f32 v22, s83, v133\n"
    "v_and_b32 v133, 0xffff0000, v127\n"
    "v_fmac_f32 v23, s83, v133\n"
    "v_lshlrev_b32 v133, 16, v128\n"
    "v_fmac_f32 v24, s83, v133\n"
    "v_and_b32 v133, 0xffff0000, v128\n"
    "v_fmac_f32 v25, s83, v133\n"
    "v_lshlrev_b32 v133, 16, v129\n"
    "v_fmac_f32 v26, s83, v133\n"
    "v_and_b32 v133, 0xffff0000, v129\n"
    "v_fmac_f32 v27, s83, v133\n"
    "v_lshlrev_b32 v133, 16, v130\n"
    "v_fmac_f32 v28, s83, v133\n"
    "v_and_b32 v133, 0xffff0000, v130\n"
    "v_fmac_f32 v29, s83, v133\n"
    "v_lshlrev_b32 v133, 16, v131\n"
    "v_fmac_f32 v30, s83, v133\n"
    "v_and_b32 v133, 0xffff0000, v131\n"
    "v_fmac_f32 v31, s83, v133\n"
    "ds_read_b64 v[124:125], v132 offset:4096\n"
    "ds_read_b64 v[126:127], v132 offset:4128\n"
    "ds_read_b64 v[128:129], v132 offset:4160\n"
    "ds_read_b64 v[130:131], v132 offset:4192\n"
    "s_waitcnt lgkmcnt(0)\n"
    "v_lshlrev_b32 v133, 16, v124\n"
    "v_fmac_f32 v32, s83, v133\n"
    "v_and_b32 v133, 0xffff0000, v124\n"
    "v_fmac_f32 v33, s83, v133\n"
    "v_lshlrev_b32 v133, 16, v125\n"
    "v_fmac_f32 v34, s83, v133\n"
    "v_and_b32 v133, 0xffff0000, v125\n"
    "v_fmac_f32 v35, s83, v133\n"
    "v_lshlrev_b32 v133, 16, v126\n"
    "v_fmac_f32 v36, s83, v133\n"
    "v_and_b32 v133, 0xffff0000, v126\n"
    "v_fmac_f32 v37, s83, v133\n"
    "v_lshlrev_b32 v133, 16, v127\n"
    "v_fmac_f32 v38, s83, v133\n"
    "v_and_b32 v133, 0xffff0000, v127\n"
    "v_fmac_f32 v39, s83, v133\n"
    "v_lshlrev_b32 v133, 16, v128\n"
    "v_fmac_f32 v40, s83, v133\n"
    "v_and_b32 v133, 0xffff0000, v128\n"
    "v_fmac_f32 v41, s83, v133\n"
    "v_lshlrev_b32 v133, 16, v129\n"
    "v_fmac_f32 v42, s83, v133\n"
    "v_and_b32 v133, 0xffff0000, v129\n"
    "v_fmac_f32 v43, s83, v133\n"
    "v_lshlrev_b32 v133, 16, v130\n"
    "v_fmac_f32 v44, s83, v133\n"
    "v_and_b32 v133, 0xffff0000, v130\n"
    "v_fmac_f32 v45, s83, v133\n"
    "v_lshlrev_b32 v133, 16, v131\n"
    "v_fmac_f32 v46, s83, v133\n"
    "v_and_b32 v133, 0xffff0000, v131\n"
    "v_fmac_f32 v47, s83, v133\n"
    "ds_read_b64 v[124:125], v132 offset:6144\n"
    "ds_read_b64 v[126:127], v132 offset:6176\n"
    "ds_read_b64 v[128:129], v132 offset:6208\n"
    "ds_read_b64 v[130:131], v132 offset:6240\n"
    "s_waitcnt lgkmcnt(0)\n"
    "v_lshlrev_b32 v133, 16, v124\n"
    "v_fmac_f32 v48, s83, v133\n"
    "v_and_b32 v133, 0xffff0000, v124\n"
    "v_fmac_f32 v49, s83, v133\n"
    "v_lshlrev_b32 v133, 16, v125\n"
    "v_fmac_f32 v50, s83, v133\n"
    "v_and_b32 v133, 0xffff0000, v125\n"
    "v_fmac_f32 v51, s83, v133\n"
    "v_lshlrev_b32 v133, 16, v126\n"
    "v_fmac_f32 v52, s83, v133\n"
    "v_and_b32 v133, 0xffff0000, v126\n"
    "v_fmac_f32 v53, s83, v133\n"
    "v_lshlrev_b32 v133, 16, v127\n"
    "v_fmac_f32 v54, s83, v133\n"
    "v_and_b32 v133, 0xffff0000, v127\n"
    "v_fmac_f32 v55, s83, v133\n"
    "v_lshlrev_b32 v133, 16, v128\n"
    "v_fmac_f32 v56, s83, v133\n"
    "v_and_b32 v133, 0xffff0000, v128\n"
    "v_fmac_f32 v57, s83, v133\n"
    "v_lshlrev_b32 v133, 16, v129\n"
    "v_fmac_f32 v58, s83, v133\n"
    "v_and_b32 v133, 0xffff0000, v129\n"
    "v_fmac_f32 v59, s83, v133\n"
    "v_lshlrev_b32 v133, 16, v130\n"
    "v_fmac_f32 v60, s83, v133\n"
    "v_and_b32 v133, 0xffff0000, v130\n"
    "v_fmac_f32 v61, s83, v133\n"
    "v_lshlrev_b32 v133, 16, v131\n"
    "v_fmac_f32 v62, s83, v133\n"
    "v_and_b32 v133, 0xffff0000, v131\n"
    "v_fmac_f32 v63, s83, v133\n"
    "s_nop 7\n"
    "s_branch .Lswin_P1R_%=\n"
    ".Lswin_P1f_%=:\n"
    "s_nop 15\n"
    "v_lshrrev_b32 v111, 8, v250\n"
    "v_bfe_u32 v110, v250, 4, 4\n"
    "v_lshlrev_b32 v110, 7, v110\n"
    "v_lshl_add_u32 v110, v111, 3, v110\n"
    "v_add_u32 v110, s92, v110\n"
    "ds_read_b64 v[102:103], v110 offset:0\n"
    "ds_read_b64 v[104:105], v110 offset:32\n"
    "ds_read_b64 v[106:107], v110 offset:64\n"
    "ds_read_b64 v[108:109], v110 offset:96\n"
    "s_waitcnt lgkmcnt(0)\n"
    "v_lshlrev_b32 v111, 16, v102\n"
    "v_fmac_f32 v0, s83, v111\n"
    "v_and_b32 v111, 0xffff0000, v102\n"
    "v_fmac_f32 v1, s83, v111\n"
    "v_lshlrev_b32 v111, 16, v103\n"
    "v_fmac_f32 v2, s83, v111\n"
    "v_and_b32 v111, 0xffff0000, v103\n"
    "v_fmac_f32 v3, s83, v111\n"
    "v_lshlrev_b32 v111, 16, v104\n"
    "v_fmac_f32 v4, s83, v111\n"
    "v_and_b32 v111, 0xffff0000, v104\n"
    "v_fmac_f32 v5, s83, v111\n"
    "v_lshlrev_b32 v111, 16, v105\n"
    "v_fmac_f32 v6, s83, v111\n"
    "v_and_b32 v111, 0xffff0000, v105\n"
    "v_fmac_f32 v7, s83, v111\n"
    "v_lshlrev_b32 v111, 16, v106\n"
    "v_fmac_f32 v8, s83, v111\n"
    "v_and_b32 v111, 0xffff0000, v106\n"
    "v_fmac_f32 v9, s83, v111\n"
    "v_lshlrev_b32 v111, 16, v107\n"
    "v_fmac_f32 v10, s83, v111\n"
    "v_and_b32 v111, 0xffff0000, v107\n"
    "v_fmac_f32 v11, s83, v111\n"
    "v_lshlrev_b32 v111, 16, v108\n"
    "v_fmac_f32 v12, s83, v111\n"
    "v_and_b32 v111, 0xffff0000, v108\n"
    "v_fmac_f32 v13, s83, v111\n"
    "v_lshlrev_b32 v111, 16, v109\n"
    "v_fmac_f32 v14, s83, v111\n"
    "v_and_b32 v111, 0xffff0000, v109\n"
    "v_fmac_f32 v15, s83, v111\n"
    "ds_read_b64 v[102:103], v110 offset:2048\n"
    "ds_read_b64 v[104:105], v110 offset:2080\n"
    "ds_read_b64 v[106:107], v110 offset:2112\n"
    "ds_read_b64 v[108:109], v110 offset:2144\n"
    "s_waitcnt lgkmcnt(0)\n"
    "v_lshlrev_b32 v111, 16, v102\n"
    "v_fmac_f32 v16, s83, v111\n"
    "v_and_b32 v111, 0xffff0000, v102\n"
    "v_fmac_f32 v17, s83, v111\n"
    "v_lshlrev_b32 v111, 16, v103\n"
    "v_fmac_f32 v18, s83, v111\n"
    "v_and_b32 v111, 0xffff0000, v103\n"
    "v_fmac_f32 v19, s83, v111\n"
    "v_lshlrev_b32 v111, 16, v104\n"
    "v_fmac_f32 v20, s83, v111\n"
    "v_and_b32 v111, 0xffff0000, v104\n"
    "v_fmac_f32 v21, s83, v111\n"
    "v_lshlrev_b32 v111, 16, v105\n"
    "v_fmac_f32 v22, s83, v111\n"
    "v_and_b32 v111, 0xffff0000, v105\n"
    "v_fmac_f32 v23, s83, v111\n"
    "v_lshlrev_b32 v111, 16, v106\n"
    "v_fmac_f32 v24, s83, v111\n"
    "v_and_b32 v111, 0xffff0000, v106\n"
    "v_fmac_f32 v25, s83, v111\n"
    "v_lshlrev_b32 v111, 16, v107\n"
    "v_fmac_f32 v26, s83, v111\n"
    "v_and_b32 v111, 0xffff0000, v107\n"
    "v_fmac_f32 v27, s83, v111\n"
    "v_lshlrev_b32 v111, 16, v108\n"
    "v_fmac_f32 v28, s83, v111\n"
    "v_and_b32 v111, 0xffff0000, v108\n"
    "v_fmac_f32 v29, s83, v111\n"
    "v_lshlrev_b32 v111, 16, v109\n"
    "v_fmac_f32 v30, s83, v111\n"
    "v_and_b32 v111, 0xffff0000, v109\n"
    "v_fmac_f32 v31, s83, v111\n"
    "ds_read_b64 v[102:103], v110 offset:4096\n"
    "ds_read_b64 v[104:105], v110 offset:4128\n"
    "ds_read_b64 v[106:107], v110 offset:4160\n"
    "ds_read_b64 v[108:109], v110 offset:4192\n"
    "s_waitcnt lgkmcnt(0)\n"
    "v_lshlrev_b32 v111, 16, v102\n"
    "v_fmac_f32 v32, s83, v111\n"
    "v_and_b32 v111, 0xffff0000, v102\n"
    "v_fmac_f32 v33, s83, v111\n"
    "v_lshlrev_b32 v111, 16, v103\n"
    "v_fmac_f32 v34, s83, v111\n"
    "v_and_b32 v111, 0xffff0000, v103\n"
    "v_fmac_f32 v35, s83, v111\n"
    "v_lshlrev_b32 v111, 16, v104\n"
    "v_fmac_f32 v36, s83, v111\n"
    "v_and_b32 v111, 0xffff0000, v104\n"
    "v_fmac_f32 v37, s83, v111\n"
    "v_lshlrev_b32 v111, 16, v105\n"
    "v_fmac_f32 v38, s83, v111\n"
    "v_and_b32 v111, 0xffff0000, v105\n"
    "v_fmac_f32 v39, s83, v111\n"
    "v_lshlrev_b32 v111, 16, v106\n"
    "v_fmac_f32 v40, s83, v111\n"
    "v_and_b32 v111, 0xffff0000, v106\n"
    "v_fmac_f32 v41, s83, v111\n"
    "v_lshlrev_b32 v111, 16, v107\n"
    "v_fmac_f32 v42, s83, v111\n"
    "v_and_b32 v111, 0xffff0000, v107\n"
    "v_fmac_f32 v43, s83, v111\n"
    "v_lshlrev_b32 v111, 16, v108\n"
    "v_fmac_f32 v44, s83, v111\n"
    "v_and_b32 v111, 0xffff0000, v108\n"
    "v_fmac_f32 v45, s83, v111\n"
    "v_lshlrev_b32 v111, 16, v109\n"
    "v_fmac_f32 v46, s83, v111\n"
    "v_and_b32 v111, 0xffff0000, v109\n"
    "v_fmac_f32 v47, s83, v111\n"
    "ds_read_b64 v[102:103], v110 offset:6144\n"
    "ds_read_b64 v[104:105], v110 offset:6176\n"
    "ds_read_b64 v[106:107], v110 offset:6208\n"
    "ds_read_b64 v[108:109], v110 offset:6240\n"
    "s_waitcnt lgkmcnt(0)\n"
    "v_lshlrev_b32 v111, 16, v102\n"
    "v_fmac_f32 v48, s83, v111\n"
    "v_and_b32 v111, 0xffff0000, v102\n"
    "v_fmac_f32 v49, s83, v111\n"
    "v_lshlrev_b32 v111, 16, v103\n"
    "v_fmac_f32 v50, s83, v111\n"
    "v_and_b32 v111, 0xffff0000, v103\n"
    "v_fmac_f32 v51, s83, v111\n"
    "v_lshlrev_b32 v111, 16, v104\n"
    "v_fmac_f32 v52, s83, v111\n"
    "v_and_b32 v111, 0xffff0000, v104\n"
    "v_fmac_f32 v53, s83, v111\n"
    "v_lshlrev_b32 v111, 16, v105\n"
    "v_fmac_f32 v54, s83, v111\n"
    "v_and_b32 v111, 0xffff0000, v105\n"
    "v_fmac_f32 v55, s83, v111\n"
    "v_lshlrev_b32 v111, 16, v106\n"
    "v_fmac_f32 v56, s83, v111\n"
    "v_and_b32 v111, 0xffff0000, v106\n"
    "v_fmac_f32 v57, s83, v111\n"
    "v_lshlrev_b32 v111, 16, v107\n"
    "v_fmac_f32 v58, s83, v111\n"
    "v_and_b32 v111, 0xffff0000, v107\n"
    "v_fmac_f32 v59, s83, v111\n"
    "v_lshlrev_b32 v111, 16, v108\n"
    "v_fmac_f32 v60, s83, v111\n"
    "v_and_b32 v111, 0xffff0000, v108\n"
    "v_fmac_f32 v61, s83, v111\n"
    "v_lshlrev_b32 v111, 16, v109\n"
    "v_fmac_f32 v62, s83, v111\n"
    "v_and_b32 v111, 0xffff0000, v109\n"
    "v_fmac_f32 v63, s83, v111\n"
    "s_nop 7\n"
    "s_branch .Lswin_P1fR_%=\n"
    ".Lswin_P2_%=:\n"
    "s_nop 15\n"
    "s_cmp_eq_u32 s84, 8\n"
    "s_cbranch_scc1 1f\n"
    "s_mov_b32 s52, s74\n"
    "s_and_b32 s53, s75, 0xffff\n"
    "s_mov_b32 s54, 16384\n"
    "v_lshrrev_b32 v62, 8, v250\n"
    "v_bfe_u32 v63, v250, 4, 4\n"
    "v_lshlrev_b32 v63, 8, v63\n"
    "v_lshl_add_u32 v63, v62, 4, v63\n"
    "s_mov_b32 s27, 0\n"
    "buffer_load_dwordx4 v[0:3], v63, s[52:55], s27 offen offset:0\n"
    "buffer_load_dwordx4 v[4:7], v63, s[52:55], s27 offen offset:64\n"
    "buffer_load_dwordx4 v[8:11], v63, s[52:55], s27 offen offset:128\n"
    "buffer_load_dwordx4 v[12:15], v63, s[52:55], s27 offen offset:192\n"
    "s_mov_b32 s27, 4096\n"
    "buffer_load_dwordx4 v[16:19], v63, s[52:55], s27 offen offset:0\n"
    "buffer_load_dwordx4 v[20:23], v63, s[52:55], s27 offen offset:64\n"
    "buffer_load_dwordx4 v[24:27], v63, s[52:55], s27 offen offset:128\n"
    "buffer_load_dwordx4 v[28:31], v63, s[52:55], s27 offen offset:192\n"
    "s_mov_b32 s27, 8192\n"
    "buffer_load_dwordx4 v[32:35], v63, s[52:55], s27 offen offset:0\n"
    "buffer_load_dwordx4 v[36:39], v63, s[52:55], s27 offen offset:64\n"
    "buffer_load_dwordx4 v[40:43], v63, s[52:55], s27 offen offset:128\n"
    "buffer_load_dwordx4 v[44:47], v63, s[52:55], s27 offen offset:192\n"
    "s_mov_b32 s27, 12288\n"
    "buffer_load_dwordx4 v[48:51], v63, s[52:55], s27 offen offset:0\n"
    "buffer_load_dwordx4 v[52:55], v63, s[52:55], s27 offen offset:64\n"
    "buffer_load_dwordx4 v[56:59], v63, s[52:55], s27 offen offset:128\n"
    "buffer_load_dwordx4 v[60:63], v63, s[52:55], s27 offen offset:192\n"
    "s_waitcnt vmcnt(0)\n"
    "s_mov_b32 s52, s72\n"
    "s_and_b32 s53, s73, 0xffff\n"
    "s_mov_b32 s54, 1800\n"
    "s_lshl_b32 s27, s26, 1\n"
    "v_lshlrev_b32 v0, 3, v0\n"
    "v_lshlrev_b32 v1, 3, v1\n"
    "v_lshlrev_b32 v2, 3, v2\n"
    "v_lshlrev_b32 v3, 3, v3\n"
    "v_lshlrev_b32 v4, 3, v4\n"
    "v_lshlrev_b32 v5, 3, v5\n"
    "v_lshlrev_b32 v6, 3, v6\n"
    "v_lshlrev_b32 v7, 3, v7\n"
    "v_lshlrev_b32 v8, 3, v8\n"
    "v_lshlrev_b32 v9, 3, v9\n"
    "v_lshlrev_b32 v10, 3, v10\n"
    "v_lshlrev_b32 v11, 3, v11\n"
    "v_lshlrev_b32 v12, 3, v12\n"
    "v_lshlrev_b32 v13, 3, v13\n"
    "v_lshlrev_b32 v14, 3, v14\n"
    "v_lshlrev_b32 v15, 3, v15\n"
    "v_lshlrev_b32 v16, 3, v16\n"
    "v_lshlrev_b32 v17, 3, v17\n"
    "v_lshlrev_b32 v18, 3, v18\n"
    "v_lshlrev_b32 v19, 3, v19\n"
    "v_lshlrev_b32 v20, 3, v20\n"
    "v_lshlrev_b32 v21, 3, v21\n"
    "v_lshlrev_b32 v22, 3, v22\n"
    "v_lshlrev_b32 v23, 3, v23\n"
    "v_lshlrev_b32 v24, 3, v24\n"
    "v_lshlrev_b32 v25, 3, v25\n"
    "v_lshlrev_b32 v26, 3, v26\n"
    "v_lshlrev_b32 v27, 3, v27\n"
    "v_lshlrev_b32 v28, 3, v28\n"
    "v_lshlrev_b32 v29, 3, v29\n"
    "v_lshlrev_b32 v30, 3, v30\n"
    "v_lshlrev_b32 v31, 3, v31\n"
    "v_lshlrev_b32 v32, 3, v32\n"
    "v_lshlrev_b32 v33, 3, v33\n"
    "v_lshlrev_b32 v34, 3, v34\n"
    "v_lshlrev_b32 v35, 3, v35\n"
    "v_lshlrev_b32 v36, 3, v36\n"
    "v_lshlrev_b32 v37, 3, v37\n"
    "v_lshlrev_b32 v38, 3, v38\n"
    "v_lshlrev_b32 v39, 3, v39\n"
    "v_lshlrev_b32 v40, 3, v40\n"
    "v_lshlrev_b32 v41, 3, v41\n"
    "v_lshlrev_b32 v42, 3, v42\n"
    "v_lshlrev_b32 v43, 3, v43\n"
    "v_lshlrev_b32 v44, 3, v44\n"
    "v_lshlrev_b32 v45, 3, v45\n"
    "v_lshlrev_b32 v46, 3, v46\n"
    "v_lshlrev_b32 v47, 3, v47\n"
    "v_lshlrev_b32 v48, 3, v48\n"
    "v_lshlrev_b32 v49, 3, v49\n"
    "v_lshlrev_b32 v50, 3, v50\n"
    "v_lshlrev_b32 v51, 3, v51\n"
    "v_lshlrev_b32 v52, 3, v52\n"
    "v_lshlrev_b32 v53, 3, v53\n"
    "v_lshlrev_b32 v54, 3, v54\n"
    "v_lshlrev_b32 v55, 3, v55\n"
    "v_lshlrev_b32 v56, 3, v56\n"
    "v_lshlrev_b32 v57, 3, v57\n"
    "v_lshlrev_b32 v58, 3, v58\n"
    "v_lshlrev_b32 v59, 3, v59\n"
    "v_lshlrev_b32 v60, 3, v60\n"
    "v_lshlrev_b32 v61, 3, v61\n"
    "v_lshlrev_b32 v62, 3, v62\n"
    "v_lshlrev_b32 v63, 3, v63\n"
    "buffer_load_ushort v0, v0, s[52:55], s27 offen\n"
    "buffer_load_ushort v1, v1, s[52:55], s27 offen\n"
    "buffer_load_ushort v2, v2, s[52:55], s27 offen\n"
    "buffer_load_ushort v3, v3, s[52:55], s27 offen\n"
    "buffer_load_ushort v4, v4, s[52:55], s27 offen\n"
    "buffer_load_ushort v5, v5, s[52:55], s27 offen\n"
    "buffer_load_ushort v6, v6, s[52:55], s27 offen\n"
    "buffer_load_ushort v7, v7, s[52:55], s27 offen\n"
    "buffer_load_ushort v8, v8, s[52:55], s27 offen\n"
    "buffer_load_ushort v9, v9, s[52:55], s27 offen\n"
    "buffer_load_ushort v10, v10, s[52:55], s27 offen\n"
    "buffer_load_ushort v11, v11, s[52:55], s27 offen\n"
    "buffer_load_ushort v12, v12, s[52:55], s27 offen\n"
    "buffer_load_ushort v13, v13, s[52:55], s27 offen\n"
    "buffer_load_ushort v14, v14, s[52:55], s27 offen\n"
    "buffer_load_ushort v15, v15, s[52:55], s27 offen\n"
    "buffer_load_ushort v16, v16, s[52:55], s27 offen\n"
    "buffer_load_ushort v17, v17, s[52:55], s27 offen\n"
    "buffer_load_ushort v18, v18, s[52:55], s27 offen\n"
    "buffer_load_ushort v19, v19, s[52:55], s27 offen\n"
    "buffer_load_ushort v20, v20, s[52:55], s27 offen\n"
    "buffer_load_ushort v21, v21, s[52:55], s27 offen\n"
    "buffer_load_ushort v22, v22, s[52:55], s27 offen\n"
    "buffer_load_ushort v23, v23, s[52:55], s27 offen\n"
    "buffer_load_ushort v24, v24, s[52:55], s27 offen\n"
    "buffer_load_ushort v25, v25, s[52:55], s27 offen\n"
    "buffer_load_ushort v26, v26, s[52:55], s27 offen\n"
    "buffer_load_ushort v27, v27, s[52:55], s27 offen\n"
    "buffer_load_ushort v28, v28, s[52:55], s27 offen\n"
    "buffer_load_ushort v29, v29, s[52:55], s27 offen\n"
    "buffer_load_ushort v30, v30, s[52:55], s27 offen\n"
    "buffer_load_ushort v31, v31, s[52:55], s27 offen\n"
    "s_waitcnt vmcnt(0)\n"
    "buffer_load_ushort v32, v32, s[52:55], s27 offen\n"
    "buffer_load_ushort v33, v33, s[52:55], s27 offen\n"
    "buffer_load_ushort v34, v34, s[52:55], s27 offen\n"
    "buffer_load_ushort v35, v35, s[52:55], s27 offen\n"
    "buffer_load_ushort v36, v36, s[52:55], s27 offen\n"
    "buffer_load_ushort v37, v37, s[52:55], s27 offen\n"
    "buffer_load_ushort v38, v38, s[52:55], s27 offen\n"
    "buffer_load_ushort v39, v39, s[52:55], s27 offen\n"
    "buffer_load_ushort v40, v40, s[52:55], s27 offen\n"
    "buffer_load_ushort v41, v41, s[52:55], s27 offen\n"
    "buffer_load_ushort v42, v42, s[52:55], s27 offen\n"
    "buffer_load_ushort v43, v43, s[52:55], s27 offen\n"
    "buffer_load_ushort v44, v44, s[52:55], s27 offen\n"
    "buffer_load_ushort v45, v45, s[52:55], s27 offen\n"
    "buffer_load_ushort v46, v46, s[52:55], s27 offen\n"
    "buffer_load_ushort v47, v47, s[52:55], s27 offen\n"
    "buffer_load_ushort v48, v48, s[52:55], s27 offen\n"
    "buffer_load_ushort v49, v49, s[52:55], s27 offen\n"
    "buffer_load_ushort v50, v50, s[52:55], s27 offen\n"
    "buffer_load_ushort v51, v51, s[52:55], s27 offen\n"
    "buffer_load_ushort v52, v52, s[52:55], s27 offen\n"
    "buffer_load_ushort v53, v53, s[52:55], s27 offen\n"
    "buffer_load_ushort v54, v54, s[52:55], s27 offen\n"
    "buffer_load_ushort v55, v55, s[52:55], s27 offen\n"
    "buffer_load_ushort v56, v56, s[52:55], s27 offen\n"
    "buffer_load_ushort v57, v57, s[52:55], s27 offen\n"
    "buffer_load_ushort v58, v58, s[52:55], s27 offen\n"
    "buffer_load_ushort v59, v59, s[52:55], s27 offen\n"
    "buffer_load_ushort v60, v60, s[52:55], s27 offen\n"
    "buffer_load_ushort v61, v61, s[52:55], s27 offen\n"
    "buffer_load_ushort v62, v62, s[52:55], s27 offen\n"
    "buffer_load_ushort v63, v63, s[52:55], s27 offen\n"
    "s_waitcnt vmcnt(0)\n"
    "v_lshlrev_b32 v0, 16, v0\n"
    "v_mul_f32 v0, s83, v0\n"
    "v_lshlrev_b32 v1, 16, v1\n"
    "v_mul_f32 v1, s83, v1\n"
    "v_lshlrev_b32 v2, 16, v2\n"
    "v_mul_f32 v2, s83, v2\n"
    "v_lshlrev_b32 v3, 16, v3\n"
    "v_mul_f32 v3, s83, v3\n"
    "v_lshlrev_b32 v4, 16, v4\n"
    "v_mul_f32 v4, s83, v4\n"
    "v_lshlrev_b32 v5, 16, v5\n"
    "v_mul_f32 v5, s83, v5\n"
    "v_lshlrev_b32 v6, 16, v6\n"
    "v_mul_f32 v6, s83, v6\n"
    "v_lshlrev_b32 v7, 16, v7\n"
    "v_mul_f32 v7, s83, v7\n"
    "v_lshlrev_b32 v8, 16, v8\n"
    "v_mul_f32 v8, s83, v8\n"
    "v_lshlrev_b32 v9, 16, v9\n"
    "v_mul_f32 v9, s83, v9\n"
    "v_lshlrev_b32 v10, 16, v10\n"
    "v_mul_f32 v10, s83, v10\n"
    "v_lshlrev_b32 v11, 16, v11\n"
    "v_mul_f32 v11, s83, v11\n"
    "v_lshlrev_b32 v12, 16, v12\n"
    "v_mul_f32 v12, s83, v12\n"
    "v_lshlrev_b32 v13, 16, v13\n"
    "v_mul_f32 v13, s83, v13\n"
    "v_lshlrev_b32 v14, 16, v14\n"
    "v_mul_f32 v14, s83, v14\n"
    "v_lshlrev_b32 v15, 16, v15\n"
    "v_mul_f32 v15, s83, v15\n"
    "v_lshlrev_b32 v16, 16, v16\n"
    "v_mul_f32 v16, s83, v16\n"
    "v_lshlrev_b32 v17, 16, v17\n"
    "v_mul_f32 v17, s83, v17\n"
    "v_lshlrev_b32 v18, 16, v18\n"
    "v_mul_f32 v18, s83, v18\n"
    "v_lshlrev_b32 v19, 16, v19\n"
    "v_mul_f32 v19, s83, v19\n"
    "v_lshlrev_b32 v20, 16, v20\n"
    "v_mul_f32 v20, s83, v20\n"
    "v_lshlrev_b32 v21, 16, v21\n"
    "v_mul_f32 v21, s83, v21\n"
    "v_lshlrev_b32 v22, 16, v22\n"
    "v_mul_f32 v22, s83, v22\n"
    "v_lshlrev_b32 v23, 16, v23\n"
    "v_mul_f32 v23, s83, v23\n"
    "v_lshlrev_b32 v24, 16, v24\n"
    "v_mul_f32 v24, s83, v24\n"
    "v_lshlrev_b32 v25, 16, v25\n"
    "v_mul_f32 v25, s83, v25\n"
    "v_lshlrev_b32 v26, 16, v26\n"
    "v_mul_f32 v26, s83, v26\n"
    "v_lshlrev_b32 v27, 16, v27\n"
    "v_mul_f32 v27, s83, v27\n"
    "v_lshlrev_b32 v28, 16, v28\n"
    "v_mul_f32 v28, s83, v28\n"
    "v_lshlrev_b32 v29, 16, v29\n"
    "v_mul_f32 v29, s83, v29\n"
    "v_lshlrev_b32 v30, 16, v30\n"
    "v_mul_f32 v30, s83, v30\n"
    "v_lshlrev_b32 v31, 16, v31\n"
    "v_mul_f32 v31, s83, v31\n"
    "v_lshlrev_b32 v32, 16, v32\n"
    "v_mul_f32 v32, s83, v32\n"
    "v_lshlrev_b32 v33, 16, v33\n"
    "v_mul_f32 v33, s83, v33\n"
    "v_lshlrev_b32 v34, 16, v34\n"
    "v_mul_f32 v34, s83, v34\n"
    "v_lshlrev_b32 v35, 16, v35\n"
    "v_mul_f32 v35, s83, v35\n"
    "v_lshlrev_b32 v36, 16, v36\n"
    "v_mul_f32 v36, s83, v36\n"
    "v_lshlrev_b32 v37, 16, v37\n"
    "v_mul_f32 v37, s83, v37\n"
    "v_lshlrev_b32 v38, 16, v38\n"
    "v_mul_f32 v38, s83, v38\n"
    "v_lshlrev_b32 v39, 16, v39\n"
    "v_mul_f32 v39, s83, v39\n"
    "v_lshlrev_b32 v40, 16, v40\n"
    "v_mul_f32 v40, s83, v40\n"
    "v_lshlrev_b32 v41, 16, v41\n"
    "v_mul_f32 v41, s83, v41\n"
    "v_lshlrev_b32 v42, 16, v42\n"
    "v_mul_f32 v42, s83, v42\n"
    "v_lshlrev_b32 v43, 16, v43\n"
    "v_mul_f32 v43, s83, v43\n"
    "v_lshlrev_b32 v44, 16, v44\n"
    "v_mul_f32 v44, s83, v44\n"
    "v_lshlrev_b32 v45, 16, v45\n"
    "v_mul_f32 v45, s83, v45\n"
    "v_lshlrev_b32 v46, 16, v46\n"
    "v_mul_f32 v46, s83, v46\n"
    "v_lshlrev_b32 v47, 16, v47\n"
    "v_mul_f32 v47, s83, v47\n"
    "v_lshlrev_b32 v48, 16, v48\n"
    "v_mul_f32 v48, s83, v48\n"
    "v_lshlrev_b32 v49, 16, v49\n"
    "v_mul_f32 v49, s83, v49\n"
    "v_lshlrev_b32 v50, 16, v50\n"
    "v_mul_f32 v50, s83, v50\n"
    "v_lshlrev_b32 v51, 16, v51\n"
    "v_mul_f32 v51, s83, v51\n"
    "v_lshlrev_b32 v52, 16, v52\n"
    "v_mul_f32 v52, s83, v52\n"
    "v_lshlrev_b32 v53, 16, v53\n"
    "v_mul_f32 v53, s83, v53\n"
    "v_lshlrev_b32 v54, 16, v54\n"
    "v_mul_f32 v54, s83, v54\n"
    "v_lshlrev_b32 v55, 16, v55\n"
    "v_mul_f32 v55, s83, v55\n"
    "v_lshlrev_b32 v56, 16, v56\n"
    "v_mul_f32 v56, s83, v56\n"
    "v_lshlrev_b32 v57, 16, v57\n"
    "v_mul_f32 v57, s83, v57\n"
    "v_lshlrev_b32 v58, 16, v58\n"
    "v_mul_f32 v58, s83, v58\n"
    "v_lshlrev_b32 v59, 16, v59\n"
    "v_mul_f32 v59, s83, v59\n"
    "v_lshlrev_b32 v60, 16, v60\n"
    "v_mul_f32 v60, s83, v60\n"
    "v_lshlrev_b32 v61, 16, v61\n"
    "v_mul_f32 v61, s83, v61\n"
    "v_lshlrev_b32 v62, 16, v62\n"
    "v_mul_f32 v62, s83, v62\n"
    "v_lshlrev_b32 v63, 16, v63\n"
    "v_mul_f32 v63, s83, v63\n"
    "1:\n"
    "s_nop 7\n"
    "s_branch .Lswin_P2R_%=\n"
    ".Lswin_P2f_%=:\n"
    "s_nop 15\n"
    "s_cmp_eq_u32 s84, 8\n"
    "s_cbranch_scc1 1f\n"
    "s_mov_b32 s52, s74\n"
    "s_and_b32 s53, s75, 0xffff\n"
    "s_mov_b32 s54, 16384\n"
    "v_lshrrev_b32 v62, 8, v250\n"
    "v_bfe_u32 v63, v250, 4, 4\n"
    "v_lshlrev_b32 v63, 8, v63\n"
    "v_lshl_add_u32 v63, v62, 4, v63\n"
    "s_mov_b32 s27, 0\n"
    "buffer_load_dwordx4 v[0:3], v63, s[52:55], s27 offen offset:0\n"
    "buffer_load_dwordx4 v[4:7], v63, s[52:55], s27 offen offset:64\n"
    "buffer_load_dwordx4 v[8:11], v63, s[52:55], s27 offen offset:128\n"
    "buffer_load_dwordx4 v[12:15], v63, s[52:55], s27 offen offset:192\n"
    "s_mov_b32 s27, 4096\n"
    "buffer_load_dwordx4 v[16:19], v63, s[52:55], s27 offen offset:0\n"
    "buffer_load_dwordx4 v[20:23], v63, s[52:55], s27 offen offset:64\n"
    "buffer_load_dwordx4 v[24:27], v63, s[52:55], s27 offen offset:128\n"
    "buffer_load_dwordx4 v[28:31], v63, s[52:55], s27 offen offset:192\n"
    "s_mov_b32 s27, 8192\n"
    "buffer_load_dwordx4 v[32:35], v63, s[52:55], s27 offen offset:0\n"
    "buffer_load_dwordx4 v[36:39], v63, s[52:55], s27 offen offset:64\n"
    "buffer_load_dwordx4 v[40:43], v63, s[52:55], s27 offen offset:128\n"
    "buffer_load_dwordx4 v[44:47], v63, s[52:55], s27 offen offset:192\n"
    "s_mov_b32 s27, 12288\n"
    "buffer_load_dwordx4 v[48:51], v63, s[52:55], s27 offen offset:0\n"
    "buffer_load_dwordx4 v[52:55], v63, s[52:55], s27 offen offset:64\n"
    "buffer_load_dwordx4 v[56:59], v63, s[52:55], s27 offen offset:128\n"
    "buffer_load_dwordx4 v[60:63], v63, s[52:55], s27 offen offset:192\n"
    "s_waitcnt vmcnt(0)\n"
    "s_mov_b32 s52, s72\n"
    "s_and_b32 s53, s73, 0xffff\n"
    "s_mov_b32 s54, 1800\n"
    "s_lshl_b32 s27, s26, 1\n"
    "v_lshlrev_b32 v0, 3, v0\n"
    "v_lshlrev_b32 v1, 3, v1\n"
    "v_lshlrev_b32 v2, 3, v2\n"
    "v_lshlrev_b32 v3, 3, v3\n"
    "v_lshlrev_b32 v4, 3, v4\n"
    "v_lshlrev_b32 v5, 3, v5\n"
    "v_lshlrev_b32 v6, 3, v6\n"
    "v_lshlrev_b32 v7, 3, v7\n"
    "v_lshlrev_b32 v8, 3, v8\n"
    "v_lshlrev_b32 v9, 3, v9\n"
    "v_lshlrev_b32 v10, 3, v10\n"
    "v_lshlrev_b32 v11, 3, v11\n"
    "v_lshlrev_b32 v12, 3, v12\n"
    "v_lshlrev_b32 v13, 3, v13\n"
    "v_lshlrev_b32 v14, 3, v14\n"
    "v_lshlrev_b32 v15, 3, v15\n"
    "v_lshlrev_b32 v16, 3, v16\n"
    "v_lshlrev_b32 v17, 3, v17\n"
    "v_lshlrev_b32 v18, 3, v18\n"
    "v_lshlrev_b32 v19, 3, v19\n"
    "v_lshlrev_b32 v20, 3, v20\n"
    "v_lshlrev_b32 v21, 3, v21\n"
    "v_lshlrev_b32 v22, 3, v22\n"
    "v_lshlrev_b32 v23, 3, v23\n"
    "v_lshlrev_b32 v24, 3, v24\n"
    "v_lshlrev_b32 v25, 3, v25\n"
    "v_lshlrev_b32 v26, 3, v26\n"
    "v_lshlrev_b32 v27, 3, v27\n"
    "v_lshlrev_b32 v28, 3, v28\n"
    "v_lshlrev_b32 v29, 3, v29\n"
    "v_lshlrev_b32 v30, 3, v30\n"
    "v_lshlrev_b32 v31, 3, v31\n"
    "v_lshlrev_b32 v32, 3, v32\n"
    "v_lshlrev_b32 v33, 3, v33\n"
    "v_lshlrev_b32 v34, 3, v34\n"
    "v_lshlrev_b32 v35, 3, v35\n"
    "v_lshlrev_b32 v36, 3, v36\n"
    "v_lshlrev_b32 v37, 3, v37\n"
    "v_lshlrev_b32 v38, 3, v38\n"
    "v_lshlrev_b32 v39, 3, v39\n"
    "v_lshlrev_b32 v40, 3, v40\n"
    "v_lshlrev_b32 v41, 3, v41\n"
    "v_lshlrev_b32 v42, 3, v42\n"
    "v_lshlrev_b32 v43, 3, v43\n"
    "v_lshlrev_b32 v44, 3, v44\n"
    "v_lshlrev_b32 v45, 3, v45\n"
    "v_lshlrev_b32 v46, 3, v46\n"
    "v_lshlrev_b32 v47, 3, v47\n"
    "v_lshlrev_b32 v48, 3, v48\n"
    "v_lshlrev_b32 v49, 3, v49\n"
    "v_lshlrev_b32 v50, 3, v50\n"
    "v_lshlrev_b32 v51, 3, v51\n"
    "v_lshlrev_b32 v52, 3, v52\n"
    "v_lshlrev_b32 v53, 3, v53\n"
    "v_lshlrev_b32 v54, 3, v54\n"
    "v_lshlrev_b32 v55, 3, v55\n"
    "v_lshlrev_b32 v56, 3, v56\n"
    "v_lshlrev_b32 v57, 3, v57\n"
    "v_lshlrev_b32 v58, 3, v58\n"
    "v_lshlrev_b32 v59, 3, v59\n"
    "v_lshlrev_b32 v60, 3, v60\n"
    "v_lshlrev_b32 v61, 3, v61\n"
    "v_lshlrev_b32 v62, 3, v62\n"
    "v_lshlrev_b32 v63, 3, v63\n"
    "buffer_load_ushort v0, v0, s[52:55], s27 offen\n"
    "buffer_load_ushort v1, v1, s[52:55], s27 offen\n"
    "buffer_load_ushort v2, v2, s[52:55], s27 offen\n"
    "buffer_load_ushort v3, v3, s[52:55], s27 offen\n"
    "buffer_load_ushort v4, v4, s[52:55], s27 offen\n"
    "buffer_load_ushort v5, v5, s[52:55], s27 offen\n"
    "buffer_load_ushort v6, v6, s[52:55], s27 offen\n"
    "buffer_load_ushort v7, v7, s[52:55], s27 offen\n"
    "buffer_load_ushort v8, v8, s[52:55], s27 offen\n"
    "buffer_load_ushort v9, v9, s[52:55], s27 offen\n"
    "buffer_load_ushort v10, v10, s[52:55], s27 offen\n"
    "buffer_load_ushort v11, v11, s[52:55], s27 offen\n"
    "buffer_load_ushort v12, v12, s[52:55], s27 offen\n"
    "buffer_load_ushort v13, v13, s[52:55], s27 offen\n"
    "buffer_load_ushort v14, v14, s[52:55], s27 offen\n"
    "buffer_load_ushort v15, v15, s[52:55], s27 offen\n"
    "buffer_load_ushort v16, v16, s[52:55], s27 offen\n"
    "buffer_load_ushort v17, v17, s[52:55], s27 offen\n"
    "buffer_load_ushort v18, v18, s[52:55], s27 offen\n"
    "buffer_load_ushort v19, v19, s[52:55], s27 offen\n"
    "buffer_load_ushort v20, v20, s[52:55], s27 offen\n"
    "buffer_load_ushort v21, v21, s[52:55], s27 offen\n"
    "buffer_load_ushort v22, v22, s[52:55], s27 offen\n"
    "buffer_load_ushort v23, v23, s[52:55], s27 offen\n"
    "buffer_load_ushort v24, v24, s[52:55], s27 offen\n"
    "buffer_load_ushort v25, v25, s[52:55], s27 offen\n"
    "buffer_load_ushort v26, v26, s[52:55], s27 offen\n"
    "buffer_load_ushort v27, v27, s[52:55], s27 offen\n"
    "buffer_load_ushort v28, v28, s[52:55], s27 offen\n"
    "buffer_load_ushort v29, v29, s[52:55], s27 offen\n"
    "buffer_load_ushort v30, v30, s[52:55], s27 offen\n"
    "buffer_load_ushort v31, v31, s[52:55], s27 offen\n"
    "s_waitcnt vmcnt(0)\n"
    "buffer_load_ushort v32, v32, s[52:55], s27 offen\n"
    "buffer_load_ushort v33, v33, s[52:55], s27 offen\n"
    "buffer_load_ushort v34, v34, s[52:55], s27 offen\n"
    "buffer_load_ushort v35, v35, s[52:55], s27 offen\n"
    "buffer_load_ushort v36, v36, s[52:55], s27 offen\n"
    "buffer_load_ushort v37, v37, s[52:55], s27 offen\n"
    "buffer_load_ushort v38, v38, s[52:55], s27 offen\n"
    "buffer_load_ushort v39, v39, s[52:55], s27 offen\n"
    "buffer_load_ushort v40, v40, s[52:55], s27 offen\n"
    "buffer_load_ushort v41, v41, s[52:55], s27 offen\n"
    "buffer_load_ushort v42, v42, s[52:55], s27 offen\n"
    "buffer_load_ushort v43, v43, s[52:55], s27 offen\n"
    "buffer_load_ushort v44, v44, s[52:55], s27 offen\n"
    "buffer_load_ushort v45, v45, s[52:55], s27 offen\n"
    "buffer_load_ushort v46, v46, s[52:55], s27 offen\n"
    "buffer_load_ushort v47, v47, s[52:55], s27 offen\n"
    "buffer_load_ushort v48, v48, s[52:55], s27 offen\n"
    "buffer_load_ushort v49, v49, s[52:55], s27 offen\n"
    "buffer_load_ushort v50, v50, s[52:55], s27 offen\n"
    "buffer_load_ushort v51, v51, s[52:55], s27 offen\n"
    "buffer_load_ushort v52, v52, s[52:55], s27 offen\n"
    "buffer_load_ushort v53, v53, s[52:55], s27 offen\n"
    "buffer_load_ushort v54, v54, s[52:55], s27 offen\n"
    "buffer_load_ushort v55, v55, s[52:55], s27 offen\n"
    "buffer_load_ushort v56, v56, s[52:55], s27 offen\n"
    "buffer_load_ushort v57, v57, s[52:55], s27 offen\n"
    "buffer_load_ushort v58, v58, s[52:55], s27 offen\n"
    "buffer_load_ushort v59, v59, s[52:55], s27 offen\n"
    "buffer_load_ushort v60, v60, s[52:55], s27 offen\n"
    "buffer_load_ushort v61, v61, s[52:55], s27 offen\n"
    "buffer_load_ushort v62, v62, s[52:55], s27 offen\n"
    "buffer_load_ushort v63, v63, s[52:55], s27 offen\n"
    "s_waitcnt vmcnt(0)\n"
    "v_lshlrev_b32 v0, 16, v0\n"
    "v_mul_f32 v0, s83, v0\n"
    "v_lshlrev_b32 v1, 16, v1\n"
    "v_mul_f32 v1, s83, v1\n"
    "v_lshlrev_b32 v2, 16, v2\n"
    "v_mul_f32 v2, s83, v2\n"
    "v_lshlrev_b32 v3, 16, v3\n"
    "v_mul_f32 v3, s83, v3\n"
    "v_lshlrev_b32 v4, 16, v4\n"
    "v_mul_f32 v4, s83, v4\n"
    "v_lshlrev_b32 v5, 16, v5\n"
    "v_mul_f32 v5, s83, v5\n"
    "v_lshlrev_b32 v6, 16, v6\n"
    "v_mul_f32 v6, s83, v6\n"
    "v_lshlrev_b32 v7, 16, v7\n"
    "v_mul_f32 v7, s83, v7\n"
    "v_lshlrev_b32 v8, 16, v8\n"
    "v_mul_f32 v8, s83, v8\n"
    "v_lshlrev_b32 v9, 16, v9\n"
    "v_mul_f32 v9, s83, v9\n"
    "v_lshlrev_b32 v10, 16, v10\n"
    "v_mul_f32 v10, s83, v10\n"
    "v_lshlrev_b32 v11, 16, v11\n"
    "v_mul_f32 v11, s83, v11\n"
    "v_lshlrev_b32 v12, 16, v12\n"
    "v_mul_f32 v12, s83, v12\n"
    "v_lshlrev_b32 v13, 16, v13\n"
    "v_mul_f32 v13, s83, v13\n"
    "v_lshlrev_b32 v14, 16, v14\n"
    "v_mul_f32 v14, s83, v14\n"
    "v_lshlrev_b32 v15, 16, v15\n"
    "v_mul_f32 v15, s83, v15\n"
    "v_lshlrev_b32 v16, 16, v16\n"
    "v_mul_f32 v16, s83, v16\n"
    "v_lshlrev_b32 v17, 16, v17\n"
    "v_mul_f32 v17, s83, v17\n"
    "v_lshlrev_b32 v18, 16, v18\n"
    "v_mul_f32 v18, s83, v18\n"
    "v_lshlrev_b32 v19, 16, v19\n"
    "v_mul_f32 v19, s83, v19\n"
    "v_lshlrev_b32 v20, 16, v20\n"
    "v_mul_f32 v20, s83, v20\n"
    "v_lshlrev_b32 v21, 16, v21\n"
    "v_mul_f32 v21, s83, v21\n"
    "v_lshlrev_b32 v22, 16, v22\n"
    "v_mul_f32 v22, s83, v22\n"
    "v_lshlrev_b32 v23, 16, v23\n"
    "v_mul_f32 v23, s83, v23\n"
    "v_lshlrev_b32 v24, 16, v24\n"
    "v_mul_f32 v24, s83, v24\n"
    "v_lshlrev_b32 v25, 16, v25\n"
    "v_mul_f32 v25, s83, v25\n"
    "v_lshlrev_b32 v26, 16, v26\n"
    "v_mul_f32 v26, s83, v26\n"
    "v_lshlrev_b32 v27, 16, v27\n"
    "v_mul_f32 v27, s83, v27\n"
    "v_lshlrev_b32 v28, 16, v28\n"
    "v_mul_f32 v28, s83, v28\n"
    "v_lshlrev_b32 v29, 16, v29\n"
    "v_mul_f32 v29, s83, v29\n"
    "v_lshlrev_b32 v30, 16, v30\n"
    "v_mul_f32 v30, s83, v30\n"
    "v_lshlrev_b32 v31, 16, v31\n"
    "v_mul_f32 v31, s83, v31\n"
    "v_lshlrev_b32 v32, 16, v32\n"
    "v_mul_f32 v32, s83, v32\n"
    "v_lshlrev_b32 v33, 16, v33\n"
    "v_mul_f32 v33, s83, v33\n"
    "v_lshlrev_b32 v34, 16, v34\n"
    "v_mul_f32 v34, s83, v34\n"
    "v_lshlrev_b32 v35, 16, v35\n"
    "v_mul_f32 v35, s83, v35\n"
    "v_lshlrev_b32 v36, 16, v36\n"
    "v_mul_f32 v36, s83, v36\n"
    "v_lshlrev_b32 v37, 16, v37\n"
    "v_mul_f32 v37, s83, v37\n"
    "v_lshlrev_b32 v38, 16, v38\n"
    "v_mul_f32 v38, s83, v38\n"
    "v_lshlrev_b32 v39, 16, v39\n"
    "v_mul_f32 v39, s83, v39\n"
    "v_lshlrev_b32 v40, 16, v40\n"
    "v_mul_f32 v40, s83, v40\n"
    "v_lshlrev_b32 v41, 16, v41\n"
    "v_mul_f32 v41, s83, v41\n"
    "v_lshlrev_b32 v42, 16, v42\n"
    "v_mul_f32 v42, s83, v42\n"
    "v_lshlrev_b32 v43, 16, v43\n"
    "v_mul_f32 v43, s83, v43\n"
    "v_lshlrev_b32 v44, 16, v44\n"
    "v_mul_f32 v44, s83, v44\n"
    "v_lshlrev_b32 v45, 16, v45\n"
    "v_mul_f32 v45, s83, v45\n"
    "v_lshlrev_b32 v46, 16, v46\n"
    "v_mul_f32 v46, s83, v46\n"
    "v_lshlrev_b32 v47, 16, v47\n"
    "v_mul_f32 v47, s83, v47\n"
    "v_lshlrev_b32 v48, 16, v48\n"
    "v_mul_f32 v48, s83, v48\n"
    "v_lshlrev_b32 v49, 16, v49\n"
    "v_mul_f32 v49, s83, v49\n"
    "v_lshlrev_b32 v50, 16, v50\n"
    "v_mul_f32 v50, s83, v50\n"
    "v_lshlrev_b32 v51, 16, v51\n"
    "v_mul_f32 v51, s83, v51\n"
    "v_lshlrev_b32 v52, 16, v52\n"
    "v_mul_f32 v52, s83, v52\n"
    "v_lshlrev_b32 v53, 16, v53\n"
    "v_mul_f32 v53, s83, v53\n"
    "v_lshlrev_b32 v54, 16, v54\n"
    "v_mul_f32 v54, s83, v54\n"
    "v_lshlrev_b32 v55, 16, v55\n"
    "v_mul_f32 v55, s83, v55\n"
    "v_lshlrev_b32 v56, 16, v56\n"
    "v_mul_f32 v56, s83, v56\n"
    "v_lshlrev_b32 v57, 16, v57\n"
    "v_mul_f32 v57, s83, v57\n"
    "v_lshlrev_b32 v58, 16, v58\n"
    "v_mul_f32 v58, s83, v58\n"
    "v_lshlrev_b32 v59, 16, v59\n"
    "v_mul_f32 v59, s83, v59\n"
    "v_lshlrev_b32 v60, 16, v60\n"
    "v_mul_f32 v60, s83, v60\n"
    "v_lshlrev_b32 v61, 16, v61\n"
    "v_mul_f32 v61, s83, v61\n"
    "v_lshlrev_b32 v62, 16, v62\n"
    "v_mul_f32 v62, s83, v62\n"
    "v_lshlrev_b32 v63, 16, v63\n"
    "v_mul_f32 v63, s83, v63\n"
    "1:\n"
    "s_nop 7\n"
    "s_branch .Lswin_P2fR_%=\n"
    ".Lswin_PW_%=:\n"
    "s_nop 15\n"
    "v_lshrrev_b32 v172, 7, v250\n"
    "v_add_u32 v172, 0, v172\n"
    "v_cmp_gt_u32_e64 s[76:77], 12, v172\n"
    "v_mov_b32 v172, 145920\n"
    "v_mov_b32 v173, -1536\n"
    "s_nop 1\n"
    "v_cndmask_b32_e64 v172, v173, v172, s[76:77]\n"
    "v_add_u32 v251, v251, v172\n"
    "v_lshrrev_b32 v172, 7, v250\n"
    "v_add_u32 v172, 8, v172\n"
    "v_cmp_gt_u32_e64 s[76:77], 12, v172\n"
    "v_mov_b32 v172, 145920\n"
    "v_mov_b32 v173, -1536\n"
    "s_nop 1\n"
    "v_cndmask_b32_e64 v172, v173, v172, s[76:77]\n"
    "v_add_u32 v249, v249, v172\n"
    "v_lshrrev_b32 v172, 7, v250\n"
    "v_add_u32 v172, 16, v172\n"
    "v_cmp_gt_u32_e64 s[76:77], 12, v172\n"
    "v_mov_b32 v172, 145920\n"
    "v_mov_b32 v173, -1536\n"
    "s_nop 1\n"
    "v_cndmask_b32_e64 v172, v173, v172, s[76:77]\n"
    "v_add_u32 v247, v247, v172\n"
    "s_nop 7\n"
    "s_branch .Lswin_PWR_%=\n"
    ".Lswin_PV_%=:\n"
    "s_nop 15\n"
    "v_lshrrev_b32 v172, 7, v250\n"
    "v_add_u32 v172, 0, v172\n"
    "v_cmp_gt_u32_e64 s[76:77], 12, v172\n"
    "v_mov_b32 v172, -145920\n"
    "v_mov_b32 v173, 1536\n"
    "s_nop 1\n"
    "v_cndmask_b32_e64 v172, v173, v172, s[76:77]\n"
    "v_add_u32 v251, v251, v172\n"
    "v_lshrrev_b32 v172, 7, v250\n"
    "v_add_u32 v172, 8, v172\n"
    "v_cmp_gt_u32_e64 s[76:77], 12, v172\n"
    "v_mov_b32 v172, -145920\n"
    "v_mov_b32 v173, 1536\n"
    "s_nop 1\n"
    "v_cndmask_b32_e64 v172, v173, v172, s[76:77]\n"
    "v_add_u32 v249, v249, v172\n"
    "v_lshrrev_b32 v172, 7, v250\n"
    "v_add_u32 v172, 16, v172\n"
    "v_cmp_gt_u32_e64 s[76:77], 12, v172\n"
    "v_mov_b32 v172, -145920\n"
    "v_mov_b32 v173, 1536\n"
    "s_nop 1\n"
    "v_cndmask_b32_e64 v172, v173, v172, s[76:77]\n"
    "v_add_u32 v247, v247, v172\n"
    "s_nop 7\n"
    "s_branch .Lswin_PVR_%=\n"
    ".Lswin_PS_%=:\n"
    "s_nop 15\n"
    "v_lshrrev_b32 v0, 7, v250\n"
    "v_add_u32 v0, 0, v0\n"
    "v_cmp_gt_u32_e64 s[76:77], 12, v0\n"
    "v_mov_b32 v0, 145920\n"
    "v_mov_b32 v1, -1536\n"
    "s_nop 1\n"
    "v_cndmask_b32_e64 v0, v1, v0, s[76:77]\n"
    "v_add_u32 v251, v251, v0\n"
    "v_lshrrev_b32 v0, 7, v250\n"
    "v_add_u32 v0, 8, v0\n"
    "v_cmp_gt_u32_e64 s[76:77], 12, v0\n"
    "v_mov_b32 v0, 145920\n"
    "v_mov_b32 v1, -1536\n"
    "s_nop 1\n"
    "v_cndmask_b32_e64 v0, v1, v0, s[76:77]\n"
    "v_add_u32 v249, v249, v0\n"
    "v_lshrrev_b32 v0, 7, v250\n"
    "v_add_u32 v0, 16, v0\n"
    "v_cmp_gt_u32_e64 s[76:77], 12, v0\n"
    "v_mov_b32 v0, 145920\n"
    "v_mov_b32 v1, -1536\n"
    "s_nop 1\n"
    "v_cndmask_b32_e64 v0, v1, v0, s[76:77]\n"
    "v_add_u32 v247, v247, v0\n"
    "s_nop 7\n"
    "s_branch .Lswin_PSR_%=\n"
    ".Lswin_END_%=:\n"
    : "=s"(bad)
    : "s"(karg), "s"(wv), "s"(blk)
    : "v0", "v1", "v2", "v3", "v4", "v5", "v6", "v7", "v8", "v9", "v10", "v11", "v12", "v13", "v14", "v15", "v16", "v17", "v18", "v19", "v20", "v21", "v22", "v23", "v24", "v25", "v26", "v27", "v28", "v29", "v30", "v31", "v32", "v33", "v34", "v35", "v36", "v37", "v38", "v39", "v40", "v41", "v42", "v43", "v44", "v45", "v46", "v47", "v48", "v49", "v50", "v51", "v52", "v53", "v54", "v55", "v56", "v57", "v58", "v59", "v60", "v61", "v62", "v63", "v64", "v65", "v66", "v67", "v68", "v69", "v70", "v71", "v72", "v73", "v74", "v75", "v76", "v77", "v78", "v79", "v80", "v81", "v82", "v83", "v84", "v85", "v86", "v87", "v88", "v89", "v90", "v91", "v92", "v93", "v94", "v95", "v96", "v97", "v98", "v99", "v100", "v101", "v102", "v103", "v104", "v105", "v106", "v107", "v108", "v109", "v110", "v111", "v112", "v113", "v114", "v115", "v116", "v117", "v118", "v119", "v120", "v121", "v122", "v123", "v124", "v125", "v126", "v127", "v128", "v129", "v130", "v131", "v132", "v133", "v134", "v135", "v136", "v137", "v138", "v139", "v140", "v141", "v142", "v143", "v144", "v145", "v146", "v147", "v148", "v149", "v150", "v151", "v152", "v153", "v154", "v155", "v156", "v157", "v158", "v159", "v160", "v161", "v162", "v163", "v164", "v165", "v166", "v167", "v168", "v169", "v170", "v171", "v172", "v173", "v174", "v175", "v176", "v177", "v178", "v179", "v180", "v181", "v182", "v183", "v184", "v185", "v186", "v187", "v188", "v189", "v190", "v191", "v192", "v193", "v194", "v195", "v196", "v197", "v198", "v199", "v200", "v201", "v202", "v203", "v204", "v205", "v206", "v207", "v208", "v209", "v210", "v211", "v212", "v213", "v214", "v215", "v216", "v217", "v218", "v219", "v220", "v221", "v222", "v223", "v224", "v225", "v226", "v227", "v228", "v229", "v230", "v231", "v232", "v233", "v234", "v235", "v236", "v237", "v238", "v239", "v240", "v241", "v242", "v243", "v244", "v245", "v246", "v247", "v248", "v249", "v250", "v251", "v252", "v253", "v254", "v255", "a0", "a1", "a2", "a3", "a4", "a5", "a6", "a7", "a8", "a9", "a10", "a11", "a12", "a13", "a14", "a15", "a16", "a17", "a18", "a19", "a20", "a21", "a22", "a23", "a24", "a25", "a26", "a27", "a28", "a29", "a30", "a31", "a32", "a33", "a34", "a35", "a36", "a37", "a38", "a39", "a40", "a41", "a42", "a43", "a44", "a45", "a46", "a47", "a48", "a49", "a50", "a51", "a52", "a53", "a54", "a55", "a56", "a57", "a58", "a59", "a60", "a61", "a62", "a63", "a64", "a65", "a66", "a67", "a68", "a69", "a70", "a71", "a72", "a73", "a74", "a75", "a76", "a77", "a78", "a79", "a80", "a81", "a82", "a83", "a84", "a85", "a86", "a87", "a88", "a89", "a90", "a91", "a92", "a93", "a94", "a95", "a96", "a97", "a98", "a99", "a100", "a101", "a102", "a103", "a104", "a105", "a106", "a107", "a108", "a109", "a110", "a111", "a112", "a113", "a114", "a115", "a116", "a117", "a118", "a119", "a120", "a121", "a122", "a123", "a124", "a125", "a126", "a127", "a128", "a129", "a130", "a131", "a132", "a133", "a134", "a135", "a136", "a137", "a138", "a139", "a140", "a141", "a142", "a143", "a144", "a145", "a146", "a147", "a148", "a149", "a150", "a151", "a152", "a153", "a154", "a155", "a156", "a157", "a158", "a159", "a160", "a161", "a162", "a163", "a164", "a165", "a166", "a167", "a168", "a169", "a170", "a171", "a172", "a173", "a174", "a175", "a176", "a177", "a178", "a179", "a180", "a181", "a182", "a183", "a184", "a185", "a186", "a187", "a188", "a189", "a190", "a191", "a192", "a193", "a194", "a195", "a196", "a197", "a198", "a199", "a200", "a201", "a202", "a203", "a204", "a205", "a206", "a207", "a208", "a209", "a210", "a211", "a212", "a213", "a214", "a215", "a216", "a217", "a218", "a219", "a220", "a221", "a222", "a223", "a224", "a225", "a226", "a227", "a228", "a229", "a230", "a231", "a232", "a233", "a234", "a235", "a236", "a237", "a238", "a239", "a240", "a241", "a242", "a243", "a244", "a245", "a246", "a247", "a248", "a249", "a250", "a251", "a252", "a253", "a254", "a255", "s20", "s21", "s22", "s23", "s24", "s25", "s26", "s27", "s28", "s29", "s30", "s31", "s34", "s35", "s36", "s37", "s38", "s39", "s40", "s41", "s42", "s43", "s44", "s45", "s46", "s47", "s48", "s49", "s50", "s51", "s52", "s53", "s54", "s55", "s56", "s57", "s58", "s59", "s60", "s61", "s62", "s63", "s64", "s65", "s66", "s67", "s68", "s69", "s70", "s71", "s72", "s73", "s74", "s75", "s76", "s77", "s78", "s79", "s80", "s81", "s82", "s83", "s84", "s85", "s86", "s87", "s88", "s89", "s90", "s91", "s92", "s93", "s94", "s95", "s96", "s97", "s98", "vcc", "scc", "memory");
  if (bad) {
    asm volatile("s_waitcnt vmcnt(0)" ::: "memory");
    unsigned long long k2 = karg; asm volatile("" : "+s"(k2));
    typedef void* const __attribute__((address_space(4)))* kp;
    const kp a = (kp)k2;
    const int tid = wv * 64 + __builtin_amdgcn_mbcnt_hi(-1, __builtin_amdgcn_mbcnt_lo(-1, 0));
    for (int i = 0; i < 9; ++i) {
      int wy, wx;
      if (i == 8 && blk < 95) { wy = blk < 48 ? 47 : blk - 48; wx = blk < 48 ? blk : 47; }
      else { int u = i < 8 ? 256 * i + blk : 2048 + blk - 95; wy = u / 47; wx = u % 47; }
      window_true_max(lds, tid, wy, wx, (const u16*)a[0], (const u16*)a[1], (const u16*)a[2], (const u16*)a[3], (const int*)a[4], (const u16*)a[5], (u16*)a[6]);
    }
  }
}

extern "C" void kernel_launch(void* const* d_in, const int* in_sizes, int n_in, void* d_out, int out_size, void* d_ws, size_t ws_size, hipStream_t stream) {
  swin_window_attn_kernel<<<256, 256, 0, stream>>>((const u16*)d_in[0], (const u16*)d_in[1], (const u16*)d_in[2], (const u16*)d_in[3],
      (const int*)d_in[4], (const u16*)d_in[5], (u16*)d_out, (unsigned*)d_ws);
}
```
